```python
import torch

def _softmax(x, axis=-1):
    return torch.softmax(x, dim=axis)
def _gelu(x):
    return torch.nn.functional.gelu(x, approximate="tanh")
def _silu(x):
    return torch.nn.functional.silu(x)
def _sigmoid(x):
    return torch.sigmoid(x)
def _layernorm(x, axis=-1, eps=1e-5):
    m = x.mean(dim=axis, keepdim=True)
    v = x.var(dim=axis, keepdim=True, unbiased=False)
    return (x - m) / torch.sqrt(v + eps)
def _scatter_add(tgt, idx, src, axis=0):
    return tgt.clone().index_add_(axis, idx, src)
def _segment_sum(src, seg, n):
    out = torch.zeros((n,) + src.shape[1:], dtype=src.dtype)
    return out.index_add_(0, seg, src)
def _pad(x, pw):
    flat = tuple(v for pair in reversed(pw) for v in pair)
    return torch.nn.functional.pad(x, flat)

kernel_name = 'concat_heads__compressed_kv_cla__sliding_block_cached__decode'

def setup_inputs():

    _w = (lambda a: (lambda t: t.to(torch.bfloat16) if t.is_floating_point() else t)(torch.as_tensor(a)))
    def _A_setup():
        B, H, Sq, Sk, D, M = 2, 32, 1, 8192, 128, 512
        Dc = 4 * D
        return {
            "x": _w((lambda *s: torch.randn(*s, dtype=torch.bfloat16) * 3.2)(B, Sq, M)),
            "c_kv": _w((lambda *s: torch.randn(*s, dtype=torch.bfloat16) * 0.1)(B, Sk, Dc)),
            "w_q": _w((lambda *s: torch.randn(*s, dtype=torch.bfloat16) * 6.4)(M, H, D) * (M ** -0.5)),
            "w_uk": _w((lambda *s: torch.randn(*s, dtype=torch.bfloat16) * 6.4)(Dc, H, D) * (Dc ** -0.5)),
            "w_uv": _w((lambda *s: torch.randn(*s, dtype=torch.bfloat16) * 0.1)(Dc, H, D) * (Dc ** -0.5)),
            "w_o": _w((lambda *s: torch.randn(*s, dtype=torch.bfloat16) * 0.1)(H, D, M) * ((H * D) ** -0.5)),
        }
    _A_in = _A_setup()

    def _B_setup():
        B, Sq, D = 2, 1, 512
        Hq, Hkv, Sk, Dh = 32, 32, 8192, 128
        assert Hq % Hkv == 0, (Hq, Hkv)
        return {
            "x": _w((lambda *s: torch.randn(*s, dtype=torch.bfloat16) * 1.6)(B, Sq, D)),
            "k_cache": _w((lambda *s: torch.randn(*s, dtype=torch.bfloat16) * 1.6)(B, Hkv, Sk, Dh)),
            "v_cache": _w((lambda *s: torch.randn(*s, dtype=torch.bfloat16) * 0.05)(B, Hkv, Sk, Dh)),
            "wq": _w((lambda *s: torch.randn(*s, dtype=torch.bfloat16) * 1.6)(D, Hq * Dh) * (D ** -0.5)),
            "wo": _w((lambda *s: torch.randn(*s, dtype=torch.bfloat16) * 0.05)(Hq * Dh, D) * ((Hq * Dh) ** -0.5)),
        }
    _B_in = _B_setup()

    return {
        "A_x": _A_in["x"],
        "A_c_kv": _A_in["c_kv"],
        "A_w_q": _A_in["w_q"],
        "A_w_uk": _A_in["w_uk"],
        "A_w_uv": _A_in["w_uv"],
        "A_w_o": _A_in["w_o"],
        "B_x": _B_in["x"],
        "B_k_cache": _B_in["k_cache"],
        "B_v_cache": _B_in["v_cache"],
        "B_wq": _B_in["wq"],
        "B_wo": _B_in["wo"],
    }

def reference(A_x, A_c_kv, A_w_q, A_w_uk, A_w_uv, A_w_o, B_x, B_k_cache, B_v_cache, B_wq, B_wo):
    def _A_ref(x, c_kv, w_q, w_uk, w_uv, w_o):
        B, Sq, M = x.shape[0], x.shape[1], x.shape[2]
        Sk = c_kv.shape[1]
        H, D = w_q.shape[1], w_q.shape[2]
        q = torch.einsum("bqm,mhd->bhqd", x, w_q)
        k = torch.einsum("bkc,chd->bhkd", c_kv, w_uk)
        v = torch.einsum("bkc,chd->bhkd", c_kv, w_uv)
        scores = torch.einsum("bhqd,bhkd->bhqk", q, k) * (D ** -0.5)
        off = Sk - Sq
        qi = torch.arange(Sq)[:, None]
        kj = torch.arange(Sk)[None, :]
        cmask = kj <= (qi + off)
        scores = torch.where(cmask[None, None], scores, -1e9)
        p = _softmax(scores, axis=-1)
        ctx = torch.einsum("bhqk,bhkd->bhqd", p, v)
        return torch.einsum("bhqd,hdm->bqm", ctx, w_o)

    def _B_ref(x, k_cache, v_cache, wq, wo):
        B, Sq, D = x.shape
        Hq, Dh = 32, 128
        Hkv, Sk = k_cache.shape[1], k_cache.shape[2]
        W = min(4096, max(256, Sk // 4))
        rep = Hq // Hkv
        q = (lambda x, *d: x.permute(*d))((x @ wq).reshape(B, Sq, Hq, Dh), 0, 2, 1, 3)
        k = (lambda x, n, axis: torch.repeat_interleave(x, n, dim=axis))(k_cache[:, :, Sk - W:, :], rep, axis=1)
        v = (lambda x, n, axis: torch.repeat_interleave(x, n, dim=axis))(v_cache[:, :, Sk - W:, :], rep, axis=1)
        scores = torch.einsum("bhqd,bhkd->bhqk", q, k) * (Dh ** -0.5)
        p = _softmax(scores, axis=-1)
        o = torch.einsum("bhqk,bhkd->bhqd", p, v)
        return (lambda x, *d: x.permute(*d))(o, 0, 2, 1, 3).reshape(B, Sq, Hq * Dh) @ wo

    a = _A_ref(A_x, A_c_kv, A_w_q, A_w_uk, A_w_uv, A_w_o)
    b = _B_ref(B_x, B_k_cache, B_v_cache, B_wq, B_wo)
    return torch.concatenate([a, b], axis=-1)
```

```cpp
#include <hip/hip_runtime.h>
#include <hip/hip_bf16.h>
#include <stdint.h>
#define ROUND_SCORES 1
typedef unsigned short u16;
__device__ __forceinline__ float bf2f(u16 x){ return __uint_as_float(((unsigned)x)<<16); }
__device__ __forceinline__ u16 f2bf(float f){ unsigned u=__float_as_uint(f); u += 0x7fffu + ((u>>16)&1u); return (u16)(u>>16); }
__device__ __forceinline__ float rbf(float f){ return bf2f(f2bf(f)); }

__global__ void k_q(const u16* x, const u16* w, float* q){
  int n = blockIdx.x*blockDim.x+threadIdx.x;
  float a0=0,a1=0;
  for(int m=0;m<512;m++){ float wv=bf2f(w[(size_t)m*4096+n]); a0+=bf2f(x[m])*wv; a1+=bf2f(x[512+m])*wv; }
  q[n]=rbf(a0); q[4096+n]=rbf(a1);
}
__global__ void k_qabs(const float* q, const u16* wuk, float* qabs){
  int idx = blockIdx.x*blockDim.x+threadIdx.x;
  int c = idx&511, h=(idx>>9)&31, b=idx>>14;
  float a=0; for(int d=0;d<128;d++) a+= q[b*4096+h*128+d]*bf2f(wuk[((size_t)c*32+h)*128+d]);
  qabs[idx]=a;
}
__global__ void k_sA(const float* qabs, const u16* ckv, float* s){
  int k = blockIdx.x*blockDim.x+threadIdx.x; int h=blockIdx.y, b=blockIdx.z;
  const u16* row = ckv + ((size_t)b*8192+k)*512; const float* qq=qabs+(b*32+h)*512;
  float a=0; for(int c=0;c<512;c++) a+=qq[c]*bf2f(row[c]);
  a = rbf(rbf(a)*0.08838834764831845f);
  s[((size_t)b*32+h)*8192+k]=a;
}
__global__ void k_sB(const float* q, const u16* kc, float* s){
  int k = blockIdx.x*blockDim.x+threadIdx.x; int h=blockIdx.y, b=blockIdx.z;
  const u16* row = kc + (((size_t)b*32+h)*8192+6144+k)*128; const float* qq=q+(b*32+h)*128;
  float a=0; for(int c=0;c<128;c++) a+=qq[c]*bf2f(row[c]);
  a = rbf(rbf(a)*0.08838834764831845f);
  s[((size_t)b*32+h)*2048+k]=a;
}
__global__ void k_softmax(float* s, int n){
  __shared__ float red[256];
  float* r = s+(size_t)blockIdx.x*n;
  float m=-INFINITY; for(int i=threadIdx.x;i<n;i+=256) m=fmaxf(m,r[i]);
  red[threadIdx.x]=m; __syncthreads();
  for(int o=128;o>0;o>>=1){ if(threadIdx.x<o) red[threadIdx.x]=fmaxf(red[threadIdx.x],red[threadIdx.x+o]); __syncthreads(); }
  m=red[0]; __syncthreads();
  float l=0; for(int i=threadIdx.x;i<n;i+=256){ float e=expf(r[i]-m); r[i]=e; l+=e; }
  red[threadIdx.x]=l; __syncthreads();
  for(int o=128;o>0;o>>=1){ if(threadIdx.x<o) red[threadIdx.x]+=red[threadIdx.x+o]; __syncthreads(); }
  l=red[0];
  for(int i=threadIdx.x;i<n;i+=256) r[i]=r[i]/l;
}
__global__ void k_pvA(const float* p, const u16* ckv, float* ctxc){
  int c = threadIdx.x + (blockIdx.x&1)*256; int h=blockIdx.x>>1, b=blockIdx.y;
  const float* pp=p+((size_t)b*32+h)*8192; float a=0;
  for(int k=0;k<8192;k++) a+=pp[k]*bf2f(ckv[((size_t)b*8192+k)*512+c]);
  ctxc[(b*32+h)*512+c]=a;
}
__global__ void k_ctxA(const float* ctxc, const u16* wuv, float* ctx){
  int idx = blockIdx.x*blockDim.x+threadIdx.x; int d=idx&127,h=(idx>>7)&31,b=idx>>12;
  float a=0; for(int c=0;c<512;c++) a+=ctxc[(b*32+h)*512+c]*bf2f(wuv[((size_t)c*32+h)*128+d]);
  ctx[idx]=rbf(a);
}
__global__ void k_pvB(const float* p, const u16* vc, float* o){
  int d=threadIdx.x, h=blockIdx.x, b=blockIdx.y;
  const float* pp=p+((size_t)b*32+h)*2048; float a=0;
  for(int k=0;k<2048;k++) a+=pp[k]*bf2f(vc[(((size_t)b*32+h)*8192+6144+k)*128+d]);
  o[(b*32+h)*128+d]=rbf(a);
}
__global__ void k_out(const float* ctx, const u16* wo, u16* out, int off){
  int m = blockIdx.x*blockDim.x+threadIdx.x; int b=blockIdx.y; float a=0;
  for(int n=0;n<4096;n++) a+=ctx[b*4096+n]*bf2f(wo[(size_t)n*512+m]);
  out[b*1024+off+m]=f2bf(a);
}
extern "C" void kernel_launch(void* const* d_in, const int* in_sizes, int n_in, void* d_out, int out_size, void* d_ws, size_t ws_size, hipStream_t stream){
  const u16* Ax=(const u16*)d_in[0]; const u16* ckv=(const u16*)d_in[1]; const u16* wq=(const u16*)d_in[2]; const u16* wuk=(const u16*)d_in[3];
  const u16* wuv=(const u16*)d_in[4]; const u16* wo=(const u16*)d_in[5]; const u16* Bx=(const u16*)d_in[6]; const u16* kc=(const u16*)d_in[7];
  const u16* vc=(const u16*)d_in[8]; const u16* Bwq=(const u16*)d_in[9]; const u16* Bwo=(const u16*)d_in[10];
  float* ws=(float*)d_ws;
  float* qA=ws; float* qB=ws+8192; float* qabs=ws+16384; float* ctxc=qabs+32768; float* ctxA=ctxc+32768; float* oB=ctxA+8192; float* sA=oB+8192; float* sB=sA+2*32*8192;
  k_q<<<16,256,0,stream>>>(Ax,wq,qA);
  k_q<<<16,256,0,stream>>>(Bx,Bwq,qB);
  k_qabs<<<128,256,0,stream>>>(qA,wuk,qabs);
  k_sA<<<dim3(32,32,2),256,0,stream>>>(qabs,ckv,sA);
  k_sB<<<dim3(8,32,2),256,0,stream>>>(qB,kc,sB);
  k_softmax<<<64,256,0,stream>>>(sA,8192);
  k_softmax<<<64,256,0,stream>>>(sB,2048);
  k_pvA<<<dim3(64,2),256,0,stream>>>(sA,ckv,ctxc);
  k_ctxA<<<32,256,0,stream>>>(ctxc,wuv,ctxA);
  k_pvB<<<dim3(32,2),128,0,stream>>>(sB,vc,oB);
  k_out<<<dim3(2,2),256,0,stream>>>(ctxA,wo,(u16*)d_out,0);
  k_out<<<dim3(2,2),256,0,stream>>>(oB,Bwo,(u16*)d_out,512);
}
```

```cpp
#include <hip/hip_runtime.h>
#include <stdint.h>

typedef unsigned short u16;
typedef unsigned long long u64;
typedef unsigned u32x4 __attribute__((ext_vector_type(4)));
typedef unsigned u32x2 __attribute__((ext_vector_type(2)));
typedef float f32x4 __attribute__((ext_vector_type(4)));
typedef short s16x4 __attribute__((ext_vector_type(4)));
typedef __attribute__((ext_vector_type(2))) __bf16 bf16x2;
typedef __attribute__((ext_vector_type(8))) __bf16 bf16x8;
#define GAS __attribute__((address_space(1)))
#define LAS __attribute__((address_space(3)))
#define DI __device__ __forceinline__
#define RLX_AGENT __ATOMIC_RELAXED, __HIP_MEMORY_SCOPE_AGENT
#define PSLEEP 4
#define THR 5

constexpr float SCALE = 0.08838834764831845f;
constexpr float LOG2E = 1.4426950408889634f;
constexpr unsigned SPIN_CAP = 1u << 14;
constexpr int NW = 8, NTHR = 576;
constexpr int LDS_BYTES = 163840;
constexpr int POOL_OFF = 12288;

constexpr int NSLOT = 11;
constexpr size_t WS_EPOCH = 0;
constexpr size_t WS_CTR   = 1024;
constexpr size_t WS_QFLAG = 4096;
constexpr size_t WS_QABS  = 8192;
constexpr size_t WS_QB    = WS_QABS + 65536;
constexpr size_t WS_QAP   = WS_QB + 65536;
constexpr size_t WS_QBP   = WS_QAP + 524288;
constexpr size_t WS_CTXP  = WS_QBP + 524288;
constexpr size_t WS_ASTAT = WS_CTXP + 131072;
constexpr size_t WS_BPART = WS_ASTAT + 65536;
constexpr size_t WS_YA    = WS_BPART + 64 * NSLOT * 144 * 8;
constexpr size_t WS_YB    = WS_YA + 524288;
constexpr size_t WS_APART = WS_YB + 524288;
constexpr size_t WS_DBG   = WS_APART + 4194304;
constexpr size_t WS_END   = WS_DBG + 65536;

DI float bf_lo(unsigned x) { return __uint_as_float(x << 16); }
DI float bf_hi(unsigned x) { return __uint_as_float(x & 0xffff0000u); }
DI float rbf(float f) { unsigned u = __float_as_uint(f); u += 0x7fffu + ((u >> 16) & 1u); return __uint_as_float(u & 0xffff0000u); }
DI unsigned f2bf(float f) { unsigned u = __float_as_uint(f); u += 0x7fffu + ((u >> 16) & 1u); return u >> 16; }
DI unsigned pack2(float lo, float hi) { return f2bf(lo) | (f2bf(hi) << 16); }
DI float dot2(unsigned a, unsigned b, float acc) {
  return __builtin_amdgcn_fdot2_f32_bf16(__builtin_bit_cast(bf16x2, a), __builtin_bit_cast(bf16x2, b), acc, false);
}
template <int CTRL> DI float dpp(float x) {
  return __builtin_bit_cast(float, __builtin_amdgcn_mov_dpp(__builtin_bit_cast(int, x), CTRL, 0xf, 0xf, true));
}
constexpr int XOR1 = 0xB1, XOR2 = 0x4E, XOR7 = 0x141, XOR8 = 0x128;
DI float row16_sum(float x) { x += dpp<XOR8>(x); x += dpp<XOR7>(x); x += dpp<XOR2>(x); x += dpp<XOR1>(x); return x; }
DI float row16_max(float x) { x = fmaxf(x, dpp<XOR8>(x)); x = fmaxf(x, dpp<XOR7>(x)); x = fmaxf(x, dpp<XOR2>(x)); x = fmaxf(x, dpp<XOR1>(x)); return x; }
DI float xrow_max(float x) {
  auto s = __builtin_amdgcn_permlane16_swap(__float_as_uint(x), __float_as_uint(x), false, false);
  x = fmaxf(__uint_as_float(s[0]), __uint_as_float(s[1]));
  auto t = __builtin_amdgcn_permlane32_swap(__float_as_uint(x), __float_as_uint(x), false, false);
  return fmaxf(__uint_as_float(t[0]), __uint_as_float(t[1]));
}
DI float xrow_sum(float x) {
  auto s = __builtin_amdgcn_permlane16_swap(__float_as_uint(x), __float_as_uint(x), false, false);
  x = __uint_as_float(s[0]) + __uint_as_float(s[1]);
  auto t = __builtin_amdgcn_permlane32_swap(__float_as_uint(x), __float_as_uint(x), false, false);
  return __uint_as_float(t[0]) + __uint_as_float(t[1]);
}
DI float x32_sum(float x) {
  auto t = __builtin_amdgcn_permlane32_swap(__float_as_uint(x), __float_as_uint(x), false, false);
  return __uint_as_float(t[0]) + __uint_as_float(t[1]);
}
DI u32x4 ldnt(const void* p) { return __builtin_nontemporal_load((const u32x4*)p); }
DI u64 ld_g(const void* p) { return __hip_atomic_load((GAS u64*)p, RLX_AGENT); }
DI void st_g(void* p, u64 v) { __hip_atomic_store((GAS u64*)p, v, RLX_AGENT); }
DI u64 mk_gran(u64 tagsh, float v) { unsigned u = __float_as_uint(v); u += 0x7fu + ((u >> 8) & 1u); return tagsh | (u64)(u >> 8); }
DI bool gran_ok(u64 g, u64 tagsh) { return (g & 0xFFFFFFFFFF000000ull) == tagsh; }
DI float gran_val(u64 g) { return __uint_as_float(((unsigned)g & 0xFFFFFFu) << 8); }
DI int swz(int row) { return ((row & 3) << 1) | (row & 8); }
DI float score_round(float d) { return rbf(rbf(d) * SCALE); }
DI float ex2(float x) { return __builtin_amdgcn_exp2f(x); }
DI void st8(float* p, const float (&a)[8]) { *(f32x4*)p = f32x4{a[0], a[1], a[2], a[3]}; *(f32x4*)(p + 4) = f32x4{a[4], a[5], a[6], a[7]}; }
DI void dma16(const void* g, char* l) {
  unsigned keep; const unsigned dst = __builtin_amdgcn_readfirstlane((unsigned)(size_t)(LAS char*)l);
  asm volatile("s_mov_b32 %0, m0\n\ts_mov_b32 m0, %2\n\ts_nop 0\n\tglobal_load_lds_dwordx4 %1, off nt\n\ts_mov_b32 m0, %0" : "=&s"(keep) : "v"(g), "s"(dst) : "memory");
}
#define VMWAIT0() asm volatile("s_waitcnt vmcnt(0)" ::: "memory")
DI f32x4 mfma16(bf16x8 a, bf16x8 b, f32x4 c) { return __builtin_amdgcn_mfma_f32_16x16x32_bf16(a, b, c, 0, 0, 0); }

#define STAMP(i) do {} while (0)

template <int ROWB> DI bf16x8 tr_frag(const char* img, int nt, int ks, int lane) {
  const int g = lane >> 4, q4 = (lane & 15) >> 2, p4 = lane & 3;
  const int row = 32 * ks + 8 * g + q4, ch = 2 * nt + (p4 >> 1);
  const char* p = img + row * ROWB + 16 * (ch ^ swz(row)) + 8 * (p4 & 1);
  const s16x4 lo = __builtin_amdgcn_ds_read_tr16_b64_v4i16((LAS s16x4*)p);
  const s16x4 hi = __builtin_amdgcn_ds_read_tr16_b64_v4i16((LAS s16x4*)(p + 4 * ROWB));
  return __builtin_bit_cast(bf16x8, __builtin_shufflevector(lo, hi, 0, 1, 2, 3, 4, 5, 6, 7));
}
template <int NC> DI bf16x8 x_frag(const char* xf, int stride, int ks, int lane) {
  const int g = lane >> 4, c = lane & 15;
  u32x4 v = {0u, 0u, 0u, 0u};
  if (c < NC) v = *(const u32x4*)(xf + c * stride + 64 * ks + 16 * g);
  return __builtin_bit_cast(bf16x8, v);
}
#define STR2(x) #x
#define STR(x) STR2(x)
#define THROTTLE() asm volatile("s_waitcnt vmcnt(" STR(THR) ")" ::: "memory")
#define DTHR THR
#define DTHROTTLE() asm volatile("s_waitcnt vmcnt(" STR(DTHR) ")" ::: "memory")
template <bool TH = true> DI void dma_rows256(const char* gbase, size_t gstride, char* img, int row0, int n, int lane) {
  const int rr = lane >> 4, pos = lane & 15;
#pragma unroll 4
  for (int i = 0; i < n; ++i) { const int row = row0 + 4 * i + rr; if (TH) DTHROTTLE(); dma16(gbase + (size_t)row * gstride + 16 * (pos ^ swz(row)), img + (row0 + 4 * i) * 256); }
}
template <bool TH = true> DI void dma_rows1k(const char* gbase, char* img, int row0, int n, int lane) {
#pragma unroll 4
  for (int i = 0; i < n; ++i) { const int row = row0 + i; if (TH) DTHROTTLE(); dma16(gbase + (size_t)row * 1024 + 16 * (lane ^ swz(row)), img + row * 1024); }
}

struct BState { float m, l; float acc[8]; };
DI unsigned cvtpk(float lo, float hi) { typedef __attribute__((ext_vector_type(2))) float f2; typedef __attribute__((ext_vector_type(2))) __bf16 b2;
  return __builtin_bit_cast(unsigned, __builtin_convertvector(f2{lo, hi}, b2)); }
DI void b_step2(const u32x4& k0, const u32x4& v0, const u32x4& k1, const u32x4& v1, const u32x4& q, BState& st) {
  float d0 = dot2(k0[0], q[0], 0.f), d1 = dot2(k1[0], q[0], 0.f);
#pragma unroll
  for (int j = 1; j < 4; ++j) { d0 = dot2(k0[j], q[j], d0); d1 = dot2(k1[j], q[j], d1); }
  d0 = row16_sum(d0); d1 = row16_sum(d1);
  unsigned r = cvtpk(d0, d1);
  r = cvtpk(bf_lo(r) * SCALE, bf_hi(r) * SCALE);
  const float s0 = bf_lo(r), s1 = bf_hi(r), sm = fmaxf(s0, s1);
  if (__any(sm > st.m)) {
    const float mn = fmaxf(st.m, sm);
    const float alpha = ex2((st.m - mn) * LOG2E);
    st.m = mn; st.l *= alpha;
#pragma unroll
    for (int j = 0; j < 8; ++j) st.acc[j] *= alpha;
  }
  const float ml = st.m * LOG2E;
  const float p0 = ex2(fmaf(s0, LOG2E, -ml)), p1 = ex2(fmaf(s1, LOG2E, -ml));
  st.l += p0 + p1;
#pragma unroll
  for (int j = 0; j < 4; ++j) {
    st.acc[2 * j] = fmaf(p1, bf_lo(v1[j]), fmaf(p0, bf_lo(v0[j]), st.acc[2 * j]));
    st.acc[2 * j + 1] = fmaf(p1, bf_hi(v1[j]), fmaf(p0, bf_hi(v0[j]), st.acc[2 * j + 1]));
  }
}
constexpr int NTICKET = 64 * NSLOT;
struct Run { int pair, slot, u0, n; };
DI Run decode_ticket(int t) {
  Run r; r.pair = t & 63; r.slot = t >> 6;
  if (r.slot == 0) { r.u0 = 0; r.n = 4; } else if (r.slot < 3) { r.u0 = 2 + 2 * r.slot; r.n = 2; } else { r.u0 = 5 + r.slot; r.n = 1; }
  return r;
}
constexpr int FIRST_DYN = 192;
DI int get_ticket(char* ws, u64 tagsh) {
#pragma unroll 1
  for (unsigned spins = 0; spins < SPIN_CAP; ++spins) {
    const u64 v = __hip_atomic_fetch_add((GAS u64*)(ws + WS_CTR), 1ull, RLX_AGENT);
    if (gran_ok(v, tagsh)) return (int)((unsigned)v & 0xFFFFFFu);
  }
  return NTICKET;
}
DI void ticket_issue(u64& tv, char* ws) {
  u64 save; tv = 1ull;
  asm volatile("s_mov_b64 %1, exec\n\ts_mov_b64 exec, 1\n\tglobal_atomic_add_x2 %0, %2, %0, %3 offset:1024 sc0\n\ts_mov_b64 exec, %1" : "+v"(tv), "=&s"(save) : "v"(0u), "s"(ws) : "memory");
}
DI int ticket_wait(u64& tv, char* ws, u64 tagsh, int lane) {
  asm volatile("s_waitcnt vmcnt(0)" : "+v"(tv) :: "memory");
  int t = 0;
  if (lane == 0) t = gran_ok(tv, tagsh) ? (int)((unsigned)tv & 0xFFFFFFu) : get_ticket(ws, tagsh);
  return __builtin_amdgcn_readfirstlane(t);
}
DI unsigned fetch_q(const char* ws, int pair, u64 tagsh, int lane) {
  const u64* qb = (const u64*)(ws + WS_QB) + pair * 128 + 2 * lane; u64 g0, g1;
#pragma unroll 1
  for (unsigned spins = 0; spins < SPIN_CAP; ++spins) { g0 = ld_g(qb); g1 = ld_g(qb + 1); if (__all(gran_ok(g0, tagsh) && gran_ok(g1, tagsh))) break; __builtin_amdgcn_s_sleep(1); }
  return (((unsigned)g0 >> 8) & 0xFFFFu) | (((unsigned)g1 << 8) & 0xFFFF0000u);
}
constexpr int PL_W = 2 * 4224, PL_Q = PL_W + 48, PL_BYTES = PL_Q + 4 * 256;
#define RAWBAR() do { asm volatile("s_waitcnt lgkmcnt(0)" ::: "memory"); __builtin_amdgcn_s_barrier(); asm volatile("" ::: "memory"); } while (0)
DI void lds_wait_ge(volatile int* w, int v) {
#pragma unroll 1
  for (unsigned spins = 0; spins < (SPIN_CAP << 4); ++spins) { if (__builtin_amdgcn_readfirstlane(*w) >= v) break; __builtin_amdgcn_s_sleep(1); }
  asm volatile("" ::: "memory");
}
DI void lds_set(volatile int* w, int v, int lane) { asm volatile("s_waitcnt lgkmcnt(0)" ::: "memory"); if (lane == 0) *w = v; }
DI void pool_helper(char* ws, char* lds, int nt, int nq, u64 tagsh, int lane) {
  volatile int* W = (volatile int*)(lds + PL_W);
  unsigned* qs = (unsigned*)(lds + PL_Q);
  bool open = __builtin_amdgcn_readfirstlane(W[(nt - 1) & 3]) < NTICKET;
#pragma unroll 1
  for (int i = -2;; ++i) {
    const bool draw = open && nt < i + 4;
    u64 tv = 0, g0 = 0, g1 = 0;
    if (draw) ticket_issue(tv, ws);
    const int tq = nq < nt ? __builtin_amdgcn_readfirstlane(W[nq & 3]) : NTICKET;
    const bool getq = tq < NTICKET;
    if (getq) { const u64* qb = (const u64*)(ws + WS_QB) + (tq & 63) * 128 + 2 * lane; g0 = ld_g(qb); g1 = ld_g(qb + 1); }
    bool last = false;
    if (i >= 0) {
      lds_wait_ge(W + 8 + (i & 1), 8 * ((i >> 1) + 1));
      const int ta = __builtin_amdgcn_readfirstlane(W[i & 3]), tb = __builtin_amdgcn_readfirstlane(W[(i + 1) & 3]);
      const float* recs = (const float*)(lds + (i & 1) * 4224);
      float M = recs[0];
#pragma unroll
      for (int w = 1; w < NW; ++w) M = fmaxf(M, recs[w * 132]);
      float o0 = 0.f, o1 = 0.f, L = 0.f;
#pragma unroll
      for (int w = 0; w < NW; ++w) { const float wt = ex2((recs[w * 132] - M) * LOG2E); o0 = fmaf(wt, recs[w * 132 + 4 + lane], o0); o1 = fmaf(wt, recs[w * 132 + 68 + lane], o1); L = fmaf(wt, recs[w * 132 + 1], L); }
      lds_set(W + 7, i + 1, lane);
      u64* slot = (u64*)(ws + WS_BPART) + ((size_t)(ta & 63) * NSLOT + (ta >> 6)) * 144;
      st_g(slot + lane, mk_gran(tagsh, o0)); st_g(slot + 64 + lane, mk_gran(tagsh, o1));
      if (lane < 2) st_g(slot + 128 + lane, mk_gran(tagsh, lane ? L : M));
      last = tb >= NTICKET;
    }
    if (last) break;
    if (draw) {
      const int t = ticket_wait(tv, ws, tagsh, lane);
      if (lane == 0) W[nt & 3] = t;
      ++nt; lds_set(W + 4, nt, lane);
      open = t < NTICKET;
    }
    if (getq) {
      unsigned qv = (((unsigned)g0 >> 8) & 0xFFFFu) | (((unsigned)g1 << 8) & 0xFFFF0000u);
      if (!__all(gran_ok(g0, tagsh) && gran_ok(g1, tagsh))) qv = fetch_q(ws, tq & 63, tagsh, lane);
      qs[64 * (nq & 3) + lane] = qv; ++nq; lds_set(W + 5, nq, lane);
    }
  }
}
#define RD 4
#define STEP_BASE(R, S) ((unsigned)(R).pair * 2097152u + (unsigned)((128 * ((R).u0 + ((S) >> 2)) + 4 * ((S) & 3)) * 256))
#define LDKV(K_, V_, OFF) asm volatile("global_load_dwordx4 %0, %2, %3 nt\n\tglobal_load_dwordx4 %1, %2, %4 nt" : "=&v"(K_), "=&v"(V_) : "v"(OFF), "s"(Kc), "s"(Vc) : "memory")
#define WAITKV(K_, V_, N) asm volatile("s_waitcnt vmcnt(%2)" : "+v"(K_), "+v"(V_) : "i"(N) : "memory")
#define WAITKV2(K0, V0, K1, V1, N) asm volatile("s_waitcnt vmcnt(%4)" : "+v"(K0), "+v"(V0), "+v"(K1), "+v"(V1) : "i"(N) : "memory")
DI unsigned kv_loff(int tid) { const int lane = tid & 63, wave = tid >> 6; return (unsigned)((6144 + 16 * wave + (lane >> 4)) * 128 + 8 * (lane & 15)) * 2u; }
DI void run_finish(BState& st, char* lds, int i, int wave, int lane) {
  volatile int* W = (volatile int*)(lds + PL_W);
  const int g = lane >> 4, c = lane & 15;
  const float Mw = xrow_max(st.m);
  const float f = ex2((st.m - Mw) * LOG2E);
#pragma unroll
  for (int j = 0; j < 8; ++j) st.acc[j] = xrow_sum(st.acc[j] * f);
  const float l = xrow_sum(st.l * f);
  if (i >= 2) lds_wait_ge(W + 7, i - 1);
  float* rec = (float*)(lds + (i & 1) * 4224) + wave * 132;
  if (g == 0) { st8(rec + 4 + 8 * c, st.acc); if (c == 0) { rec[0] = Mw; rec[1] = l; } }
  asm volatile("s_waitcnt lgkmcnt(0)" ::: "memory");
  if (lane == 0) __hip_atomic_fetch_add((LAS int*)(LAS char*)(lds + PL_W + 32 + 4 * (i & 1)), 1, __ATOMIC_RELAXED, __HIP_MEMORY_SCOPE_WORKGROUP);
  st.m = -INFINITY; st.l = 0.f;
#pragma unroll
  for (int j = 0; j < 8; ++j) st.acc[j] = 0.f;
}
template <bool PRE>
DI void pool_stream(const u16* Kc, const u16* Vc, char* lds, int i0, int t_cur, int tid, u32x4 (&pk)[8], u32x4 (&pv)[8]) {
  const int lane = tid & 63, wave = __builtin_amdgcn_readfirstlane(tid >> 6), c = lane & 15;
  const unsigned loff = kv_loff(tid);
  volatile int* W = (volatile int*)(lds + PL_W);
  const char* qs = lds + PL_Q;
  u32x4 kr[RD], vr[RD];
  Run cur = decode_ticket(t_cur);
  const bool valid = t_cur < NTICKET;
#pragma unroll
  for (int i = 0; i < RD; ++i) LDKV(kr[i], vr[i], valid ? STEP_BASE(cur, i) + loff : 0u);
  int ri = i0, s = 0, ns = 4 * cur.n;
  BState st; st.m = -INFINITY; st.l = 0.f;
#pragma unroll
  for (int j = 0; j < 8; ++j) st.acc[j] = 0.f;
  if (PRE) {
    const u32x4 q0 = *(const u32x4*)(qs + 16 * c);
#pragma unroll
    for (int i = 0; i < 8; i += 2) { if (i < 6) WAITKV2(pk[i], pv[i], pk[i + 1], pv[i + 1], 4 + 2 * RD); else WAITKV2(pk[i], pv[i], pk[i + 1], pv[i + 1], 2 * RD); b_step2(pk[i], pv[i], pk[i + 1], pv[i + 1], q0, st); }
    run_finish(st, lds, 0, wave, lane);
    if (!valid) { asm volatile("s_waitcnt vmcnt(0)" ::: "memory"); return; }
  }
  lds_wait_ge(W + 5, ri + 1);
  u32x4 q = *(const u32x4*)(qs + 256 * (ri & 3) + 16 * c);
#pragma unroll 1
  for (;;) {
    const bool lastblk = (s + RD == ns);
    bool more = true; Run nx = cur;
    if (lastblk) { lds_wait_ge(W + 4, ri + 2); const int t = __builtin_amdgcn_readfirstlane(W[(ri + 1) & 3]); more = t < NTICKET; nx = decode_ticket(t); }
#pragma unroll
    for (int i = 0; i < RD; i += 2) {
      WAITKV2(kr[i], vr[i], kr[i + 1], vr[i + 1], 2 * (RD - 2));
      b_step2(kr[i], vr[i], kr[i + 1], vr[i + 1], q, st);
      __builtin_amdgcn_sched_barrier(0);
      const unsigned o = !lastblk ? STEP_BASE(cur, s + i + RD) + loff : (more ? STEP_BASE(nx, i) + loff : 0u);
      LDKV(kr[i], vr[i], o);
      LDKV(kr[i + 1], vr[i + 1], (!lastblk || more) ? o + 1024u : 0u);
    }
    s += RD;
    if (lastblk) {
      run_finish(st, lds, ri, wave, lane);
      if (!more) break;
      cur = nx; s = 0; ns = 4 * cur.n; ++ri;
      lds_wait_ge(W + 5, ri + 1);
      q = *(const u32x4*)(qs + 256 * (ri & 3) + 16 * c);
    }
  }
  asm volatile("s_waitcnt vmcnt(0)" ::: "memory");
}

DI void stage1_compute(const char* img, const char* xf, u64* dst, u64 tagsh, int tid) {
  const int lane = tid & 63, wave = tid >> 6;
  f32x4 d = mfma16(tr_frag<256>(img, wave, 0, lane), x_frag<2>(xf, 128, 0, lane), f32x4{0.f, 0.f, 0.f, 0.f});
  d = mfma16(tr_frag<256>(img, wave, 1, lane), x_frag<2>(xf, 128, 1, lane), d);
  const int b = (lane >> 4) & 1, n = lane & 15, srcl = 16 * (n >> 2) + b;
  float v = 0.f;
#pragma unroll
  for (int r = 0; r < 4; ++r) { const float t0 = __shfl(d[r], srcl); if ((n & 3) == r) v = t0; }
  if (lane < 32) st_g(dst + b * 128 + 16 * wave + n, mk_gran(tagsh, v));
}

DI void out_stage(const float* ybuf, float* red, size_t yoff, const __amdgpu_buffer_rsrc_t& wsr, u16* out, int srv, int colbase, u64 tagsh, int tid) {
  const int lane = tid & 63, wave = tid >> 6;
  {
    const u64 ga = mk_gran(tagsh, ybuf[2 * tid]), gb = mk_gran(tagsh, ybuf[2 * tid + 1]);
    __builtin_amdgcn_raw_buffer_store_b128(u32x4{(unsigned)ga, (unsigned)(ga >> 32), (unsigned)gb, (unsigned)(gb >> 32)}, wsr, (unsigned)(yoff + (size_t)srv * 8192 + 16 * tid), 0, 16);
  }
  const int b = srv >> 5, m0 = (srv & 31) * 16;
  const unsigned off = (unsigned)(yoff + (size_t)(tid >> 3) * 8192 + (b * 512 + m0 + 2 * (tid & 7)) * 8);
  const unsigned th = (unsigned)(tagsh >> 32), tl = (unsigned)tagsh;
  u32x4 v; unsigned spins = 0;
#pragma unroll 1
  for (;;) {
    v = __builtin_amdgcn_raw_buffer_load_b128(wsr, off, 0, 16);
    if (__all(v[1] == th && (v[0] & 0xFF000000u) == tl && v[3] == th && (v[2] & 0xFF000000u) == tl)) break;
    if (++spins > SPIN_CAP) break;
    __builtin_amdgcn_s_sleep(1);
  }
  float f0 = __uint_as_float(v[0] << 8), f1 = __uint_as_float(v[2] << 8);
  f0 += dpp<XOR8>(f0); f1 += dpp<XOR8>(f1);
  f0 = xrow_sum(f0); f1 = xrow_sum(f1);
  if (lane < 8) { red[wave * 16 + 2 * lane] = f0; red[wave * 16 + 2 * lane + 1] = f1; }
  __syncthreads();
  if (tid < 16) {
    float s = 0.f;
#pragma unroll
    for (int w = 0; w < NW; ++w) s += red[w * 16 + tid];
    out[b * 1024 + colbase + m0 + tid] = (u16)f2bf(s);
  }
}

__global__ void __launch_bounds__(NTHR) fused_kernel(
    const u16* __restrict__ Ax, const u16* __restrict__ ckv, const u16* __restrict__ Awq, const u16* __restrict__ Awuk,
    const u16* __restrict__ Awuv, const u16* __restrict__ Awo, const u16* __restrict__ Bx, const u16* __restrict__ Kc,
    const u16* __restrict__ Vc, const u16* __restrict__ Bwq, const u16* __restrict__ Bwo, u16* __restrict__ out, char* __restrict__ ws) {
  extern __shared__ __attribute__((aligned(16))) char smem[];
  const int tid = threadIdx.x, lane = tid & 63, wave = __builtin_amdgcn_readfirstlane(tid >> 6);
  const int g = lane >> 4, c = lane & 15;
  const int bid = blockIdx.x, xg = bid & 7, idx = bid >> 3;
  asm volatile("" :: "s"(Ax), "s"(ckv), "s"(Awq), "s"(Awuk), "s"(Awuv), "s"(Awo), "s"(Bx), "s"(Kc), "s"(Vc), "s"(Bwq), "s"(Bwo), "s"(out), "s"(ws));
  STAMP(0);
#define GET_TAG() u64 epoch; asm volatile("s_load_dwordx2 %0, %1, 0x0\n\ts_waitcnt lgkmcnt(0)" : "=&s"(epoch) : "s"(ws) : "memory"); const u64 tagsh = (epoch + 1) << 24
  const __amdgpu_buffer_rsrc_t wsr = __builtin_amdgcn_make_buffer_rsrc(ws, 0, (int)WS_END, 0x00020000);
  char* aux = smem + 131072;
  if (wave == 8) {
    if (idx < 8) return;
    GET_TAG();
    char* pl = aux + POOL_OFF;
    volatile int* W = (volatile int*)(pl + PL_W);
    __builtin_amdgcn_s_barrier(); __builtin_amdgcn_s_barrier(); __builtin_amdgcn_s_barrier();
    if (idx < 16) {
      const int t0 = xg * 8 + idx - 8;
      if (lane < 12) W[lane] = lane == 0 ? t0 : (lane == 4 ? 1 : 0);
      RAWBAR();
      pool_helper(ws, pl, 1, 0, tagsh, lane);
    } else {
      const int t0 = 64 + xg * 16 + idx - 16;
      const u64* fp = (const u64*)(ws + WS_QFLAG) + xg * 64 + lane; unsigned spins = 0;
#pragma unroll 1
      for (;;) { const u64 g0 = ld_g(fp); if (__all(gran_ok(g0, tagsh))) break; if (++spins > SPIN_CAP) break; __builtin_amdgcn_s_sleep(2); }
      __builtin_amdgcn_s_barrier();
      const u64* qb0 = (const u64*)(ws + WS_QB) + (t0 & 63) * 128 + 2 * lane;
      const u64 a0 = ld_g(qb0), a1 = ld_g(qb0 + 1);
      __builtin_amdgcn_s_barrier();
      __builtin_amdgcn_s_barrier();
      u64 tv; ticket_issue(tv, ws);
      __builtin_amdgcn_s_barrier(); __builtin_amdgcn_s_barrier(); __builtin_amdgcn_s_barrier(); __builtin_amdgcn_s_barrier();
      const int t1 = ticket_wait(tv, ws, tagsh, lane);
      unsigned q0 = (((unsigned)a0 >> 8) & 0xFFFFu) | (((unsigned)a1 << 8) & 0xFFFF0000u);
      if (!__all(gran_ok(a0, tagsh) && gran_ok(a1, tagsh))) q0 = fetch_q(ws, t0 & 63, tagsh, lane);
      ((unsigned*)(pl + PL_Q))[lane] = q0;
      if (lane < 12) W[lane] = lane == 0 ? t0 : (lane == 1 ? t1 : (lane == 4 ? 2 : (lane == 5 ? 1 : 0)));
      RAWBAR();
      pool_helper(ws, pl, 2, 1, tagsh, lane);
    }
    return;
  }
  char* s1img = aux;
  char* s1x = smem + 131072 - 512;
  const int h1 = 4 * xg + (idx >> 3), j1 = idx & 7;
  if (tid < 32) dma16((tid < 16 ? Ax : Bx) + ((tid >> 3) & 1) * 512 + 64 * j1 + 8 * (tid & 7), s1x);
  dma_rows256<false>((const char*)(Awq + ((size_t)64 * j1 * 32 + h1) * 128), 8192, s1img, 8 * wave, 2, lane);
  dma_rows256<false>((const char*)(Bwq + (size_t)64 * j1 * 4096 + h1 * 128), 8192, s1img + 16384, 8 * wave, 2, lane);
#define S1_WAIT(N) do { asm volatile("s_waitcnt vmcnt(%0) lgkmcnt(0)" :: "i"(N) : "memory"); __builtin_amdgcn_s_barrier(); asm volatile("" ::: "memory"); } while (0)
#define STAGE1(NL) do { S1_WAIT((NL) + 2); stage1_compute(s1img, s1x, (u64*)(ws + WS_QAP) + ((size_t)h1 * 8 + j1) * 256, tagsh, tid); \
    S1_WAIT((NL) + 1); stage1_compute(s1img + 16384, s1x + 256, (u64*)(ws + WS_QBP) + ((size_t)h1 * 8 + j1) * 256, tagsh, tid); RAWBAR(); } while (0)

  if (idx < 8) {
    const int h = 4 * xg + (idx >> 1), half = idx & 1, srv = h * 2 + half;
    char* xf = aux;
    char* qf = aux + 4224;
    char* xf2 = aux + 4864;
    float* rec = (float*)(aux + 5376);
    float* ybuf = (float*)(aux + 13824);
    float* red = (float*)(aux + 17920);
    dma_rows256<false>((const char*)(Awuk + ((size_t)256 * half * 32 + h) * 128), 8192, smem, 32 * wave, 4, lane);
    GET_TAG();
    STAGE1(4);
    dma_rows256<false>((const char*)(Awuk + ((size_t)256 * half * 32 + h) * 128), 8192, smem, 32 * wave + 16, 4, lane);
    if (srv == 0 && tid == 0) st_g(ws + WS_CTR, tagsh | (u64)FIRST_DYN);
    VMWAIT0();
    STAMP(8);
#define SUM8(QP, S) do { u64 gq[8]; unsigned spins = 0; \
      for (;;) { bool ok = true; _Pragma("unroll") for (int k = 0; k < 8; ++k) gq[k] = ld_g((QP) + k * 256); \
        _Pragma("unroll") for (int k = 0; k < 8; ++k) ok = ok && gran_ok(gq[k], tagsh); \
        if (__all(ok)) break; if (++spins > SPIN_CAP) break; __builtin_amdgcn_s_sleep(1); } \
      S = 0.f; _Pragma("unroll") for (int k = 0; k < 8; ++k) S += gran_val(gq[k]); } while (0)
    if (wave < 4) {
      float s; SUM8((const u64*)(ws + WS_QAP) + (size_t)h * 2048 + tid, s);
      *(u16*)(qf + (tid >> 7) * 272 + 2 * (tid & 127)) = (u16)f2bf(s);
    }
    __syncthreads();
    STAMP(10);
    {
      bf16x8 qb[4];
#pragma unroll
      for (int s = 0; s < 4; ++s) qb[s] = x_frag<2>(qf, 272, s, lane);
#pragma unroll
      for (int t = 0; t < 2; ++t) {
        f32x4 d = {0.f, 0.f, 0.f, 0.f};
#pragma unroll
        for (int s = 0; s < 4; ++s) d = mfma16(*(const bf16x8*)(smem + (32 * wave + 16 * t + c) * 256 + 16 * ((4 * s + g) ^ swz(c))), qb[s], d);
        if (c < 2) {
          const size_t off = WS_QABS + ((((size_t)c * 2 + (h >> 4)) * 16 + 8 * half + wave) * 64 + (2 * t + (g >> 1)) * 16 + (h & 15)) * 16 + 8 * (g & 1);
          st_g(ws + off, (u64)pack2(d[0], d[1]) | ((u64)pack2(d[2], d[3]) << 32));
        }
      }
      asm volatile("s_waitcnt vmcnt(0)" ::: "memory");
    }
    __syncthreads();
    if (tid < 8) st_g(ws + WS_QFLAG + tid * 512 + srv * 8, tagsh | 0x5A5A5Aull);
    STAMP(1);
    if (wave == 4 || wave == 5) {
      float s; SUM8((const u64*)(ws + WS_QBP) + (size_t)h * 2048 + half * 128 + (tid - 256), s);
      st_g((u64*)(ws + WS_QB) + (half * 32 + h) * 128 + (tid - 256), mk_gran(tagsh, rbf(s)));
    }
    dma_rows1k<false>((const char*)(Awo + ((size_t)h * 128 + 64 * half) * 512), smem + 65536, 8 * wave, 8, lane);
    dma_rows256<false>((const char*)(Awuv + ((size_t)256 * half * 32 + h) * 128), 8192, smem, 32 * wave, 8, lane);
    S1_WAIT(8);
    bf16x8 wof[4][2];
#pragma unroll
    for (int t = 0; t < 4; ++t) { wof[t][0] = tr_frag<1024>(smem + 65536, 4 * wave + t, 0, lane); wof[t][1] = tr_frag<1024>(smem + 65536, 4 * wave + t, 1, lane); }
    RAWBAR();
    dma_rows1k<false>((const char*)(Bwo + ((size_t)h * 128 + 64 * half) * 512), smem + 65536, 8 * wave, 8, lane);
    {
      const int b = wave >> 2, ck0 = 16 * (wave & 3);
      const u64* sp = (const u64*)(ws + WS_ASTAT) + (((size_t)h * 2 + b) * 64 + ck0) * 2 + (lane & 31);
      u64 gs; unsigned spins = 0;
#pragma unroll 1
      for (;;) { gs = ld_g(sp); if (__all(gran_ok(gs, tagsh))) break; if (++spins > SPIN_CAP) break; __builtin_amdgcn_s_sleep(PSLEEP); }
      __builtin_amdgcn_fence(__ATOMIC_ACQUIRE, "wavefront");
      STAMP(9);
      const int hi = lane >> 5, l5 = lane & 31;
      u32x4 pr[8];
#pragma unroll
      for (int i = 0; i < 8; ++i)
        pr[i] = __builtin_amdgcn_raw_buffer_load_b128(wsr, (unsigned)(WS_APART + ((((size_t)b * 32 + h) * 64 + ck0 + 2 * i + hi) * 512 + 256 * half + 8 * l5) * 2), 0, 16);
      __builtin_amdgcn_sched_barrier(0);
      const float sv = gran_val(gs);
      const float Mw = xrow_max(row16_max((lane & 1) ? -INFINITY : sv));
      const float we = ex2((sv - Mw) * LOG2E);
      const float wl = dpp<XOR1>(we) * sv;
      const float Lw = xrow_sum(row16_sum(((lane & 1) && lane < 32) ? wl : 0.f));
      float a[8];
#pragma unroll
      for (int j = 0; j < 8; ++j) a[j] = 0.f;
#pragma unroll
      for (int i = 0; i < 8; ++i) {
        const float wlo = __builtin_bit_cast(float, __builtin_amdgcn_readlane(__builtin_bit_cast(int, we), 4 * i));
        const float whi = __builtin_bit_cast(float, __builtin_amdgcn_readlane(__builtin_bit_cast(int, we), 4 * i + 2));
        const float wt = hi ? whi : wlo;
#pragma unroll
        for (int j = 0; j < 4; ++j) { a[2 * j] = fmaf(wt, bf_lo(pr[i][j]), a[2 * j]); a[2 * j + 1] = fmaf(wt, bf_hi(pr[i][j]), a[2 * j + 1]); }
      }
#pragma unroll
      for (int j = 0; j < 8; ++j) a[j] = x32_sum(a[j]);
      float* rc = rec + wave * 260;
      if (hi == 0) st8(rc + 4 + 8 * l5, a);
      if (lane == 0) { rc[0] = Mw; rc[1] = Lw; }
    }
    VMWAIT0();
    __syncthreads();
    STAMP(2);
    {
      const int bb = tid >> 8, c2 = tid & 255;
      const float* r0 = rec + (bb * 4) * 260;
      const float M = fmaxf(fmaxf(r0[0], r0[260]), fmaxf(r0[520], r0[780]));
      float o = 0.f, L = 0.f;
#pragma unroll
      for (int w = 0; w < 4; ++w) { const float ew = ex2((r0[w * 260] - M) * LOG2E); o = fmaf(ew, r0[w * 260 + 4 + c2], o); L = fmaf(ew, r0[w * 260 + 1], L); }
      const float v = o / L;
      const float vh = rbf(v);
      *(u16*)(xf + bb * 1040 + 2 * c2) = (u16)(__float_as_uint(vh) >> 16);
      *(u16*)(xf + (2 + bb) * 1040 + 2 * c2) = (u16)f2bf(v - vh);
    }
    __syncthreads();
    {
      f32x4 d = {0.f, 0.f, 0.f, 0.f};
#pragma unroll
      for (int ks = 0; ks < 8; ++ks) d = mfma16(tr_frag<256>(smem, wave, ks, lane), x_frag<4>(xf, 1040, ks, lane), d);
#pragma unroll
      for (int j = 0; j < 4; ++j) d[j] += dpp<XOR2>(d[j]);
      u64* cp = (u64*)(ws + WS_CTXP);
      const int dd = 16 * wave + 4 * g;
      if ((wave >> 2) != half) {
        if (c < 2) {
#pragma unroll
          for (int j = 0; j < 4; ++j) st_g(cp + (size_t)srv * 256 + c * 128 + dd + j, mk_gran(tagsh, d[j]));
        }
      } else {
        const u64* pp = cp + (size_t)(srv ^ 1) * 256 + (c & 1) * 128 + dd; u64 gx[4]; unsigned spins = 0;
        for (;;) {
#pragma unroll
          for (int j = 0; j < 4; ++j) gx[j] = ld_g(pp + j);
          if (__all(gran_ok(gx[0], tagsh) && gran_ok(gx[1], tagsh) && gran_ok(gx[2], tagsh) && gran_ok(gx[3], tagsh))) break;
          if (++spins > SPIN_CAP) break;
        }
        if (c < 2) *(u32x2*)(xf2 + c * 144 + (dd & 63) * 2) = u32x2{pack2(d[0] + gran_val(gx[0]), d[1] + gran_val(gx[1])), pack2(d[2] + gran_val(gx[2]), d[3] + gran_val(gx[3]))};
      }
    }
    __syncthreads();
    STAMP(3);
    {
      const bf16x8 b0 = x_frag<2>(xf2, 144, 0, lane), b1 = x_frag<2>(xf2, 144, 1, lane);
#pragma unroll
      for (int t = 0; t < 4; ++t) {
        f32x4 d = mfma16(wof[t][0], b0, f32x4{0.f, 0.f, 0.f, 0.f}); d = mfma16(wof[t][1], b1, d);
        if (c < 2) *(f32x4*)(ybuf + c * 512 + 16 * (4 * wave + t) + 4 * g) = d;
      }
    }
    __syncthreads();
    out_stage(ybuf, red, WS_YA, wsr, out, srv, 0, tagsh, tid);
    STAMP(4);
#pragma unroll
    for (int t = 0; t < 4; ++t) { wof[t][0] = tr_frag<1024>(smem + 65536, 4 * wave + t, 0, lane); wof[t][1] = tr_frag<1024>(smem + 65536, 4 * wave + t, 1, lane); }
    if (wave < 2) {
      const int b = wave;
      const u64* base = (const u64*)(ws + WS_BPART) + (size_t)(b * 32 + h) * NSLOT * 144;
      u64 go[NSLOT], gs; unsigned spins = 0;
      const int sl = min(lane >> 1, NSLOT - 1);
#pragma unroll 1
      for (;;) { gs = ld_g(base + sl * 144 + 128 + (lane & 1)); if (__all(gran_ok(gs, tagsh))) break; if (++spins > SPIN_CAP) break; __builtin_amdgcn_s_sleep(PSLEEP); }
#pragma unroll 1
      for (;;) {
#pragma unroll
        for (int s = 0; s < NSLOT; ++s) go[s] = ld_g(base + s * 144 + 64 * half + lane);
        bool ok = true;
#pragma unroll
        for (int s = 0; s < NSLOT; ++s) ok = ok && gran_ok(go[s], tagsh);
        if (__all(ok)) break;
        if (++spins > SPIN_CAP) break;
      }
      const float sv = gran_val(gs);
      const float M = xrow_max(row16_max((lane & 1) ? -INFINITY : sv));
      const float we = ex2((sv - M) * LOG2E);
      const float wl = dpp<XOR1>(we) * sv;
      const float L = xrow_sum(row16_sum(((lane & 1) && lane < 2 * NSLOT) ? wl : 0.f));
      float o = 0.f;
#pragma unroll
      for (int s = 0; s < NSLOT; ++s) o = fmaf(__builtin_bit_cast(float, __builtin_amdgcn_readlane(__builtin_bit_cast(int, we), 2 * s)), gran_val(go[s]), o);
      o /= L;
      *(u16*)(xf2 + b * 144 + 2 * lane) = (u16)f2bf(o);
    }
    __syncthreads();
    STAMP(5);
    {
      const bf16x8 b0 = x_frag<2>(xf2, 144, 0, lane), b1 = x_frag<2>(xf2, 144, 1, lane);
#pragma unroll
      for (int t = 0; t < 4; ++t) {
        f32x4 d = mfma16(wof[t][0], b0, f32x4{0.f, 0.f, 0.f, 0.f}); d = mfma16(wof[t][1], b1, d);
        if (c < 2) *(f32x4*)(ybuf + c * 512 + 16 * (4 * wave + t) + 4 * g) = d;
      }
    }
    __syncthreads();
    out_stage(ybuf, red, WS_YB, wsr, out, srv, 512, tagsh, tid);
    if (srv == 0 && tid == 0) st_g(ws + WS_EPOCH, epoch + 1);
    STAMP(7);
  } else if (idx < 16) {
    GET_TAG();
    STAGE1(0);
    RAWBAR();
    STAMP(1);
    { u32x4 dk[8], dv[8]; pool_stream<false>(Kc, Vc, aux + POOL_OFF, 0, xg * 8 + idx - 8, tid, dk, dv); }
    STAMP(3);
  } else {
    const int a16 = idx - 16, a = xg * 16 + a16, b = a >> 6, ck = a & 63;
    dma_rows1k<false>((const char*)(ckv + ((size_t)b * 8192 + ck * 128) * 512), smem, 16 * wave, 2, lane);
    GET_TAG();
    STAGE1(2);
    dma_rows1k((const char*)(ckv + ((size_t)b * 8192 + ck * 128) * 512), smem, 16 * wave + 2, 14, lane);
    STAMP(7);
    VMWAIT0();
    __syncthreads();
    STAMP(1);
    {
      u32x4 t[4];
#pragma unroll
      for (int i = 0; i < 4; ++i) t[i] = __builtin_amdgcn_raw_buffer_load_b128(wsr, (unsigned)(WS_QABS + b * 32768 + (4 * wave + i) * 1024 + 16 * lane), 0, 16);
#pragma unroll
      for (int i = 0; i < 4; ++i) *(u32x4*)(aux + (4 * wave + i) * 1024 + 16 * lane) = t[i];
    }
    u32x4 pk[8], pv[8];
    const Run r0 = decode_ticket(64 + a); const unsigned loff0 = kv_loff(tid);
    __syncthreads();
#pragma unroll
    for (int i = 0; i < 2; ++i) LDKV(pk[i], pv[i], STEP_BASE(r0, i) + loff0);
    const int kp = wave >> 1, hh = wave & 1;
    STAMP(2);
    f32x4 s0 = {0.f, 0.f, 0.f, 0.f}, s1 = {0.f, 0.f, 0.f, 0.f};
    {
      const int f = swz(c);
      const char* rb = smem + (32 * kp + c) * 1024;
      const char* qb = aux + hh * 16384 + 16 * lane;
#pragma unroll 4
      for (int s = 0; s < 16; ++s) {
        const bf16x8 qfr = *(const bf16x8*)(qb + s * 1024);
        const bf16x8 af0 = *(const bf16x8*)(rb + 256 * (s >> 2) + 16 * ((4 * (s & 3) + g) ^ f));
        const bf16x8 af1 = *(const bf16x8*)(rb + 16384 + 256 * (s >> 2) + 16 * ((4 * (s & 3) + g) ^ f));
        s0 = mfma16(af0, qfr, s0);
        s1 = mfma16(af1, qfr, s1);
      }
    }
    float sc[8];
#pragma unroll
    for (int j = 0; j < 4; ++j) { sc[j] = score_round(s0[j]); sc[4 + j] = score_round(s1[j]); }
    float tm = fmaxf(fmaxf(fmaxf(sc[0], sc[1]), fmaxf(sc[2], sc[3])), fmaxf(fmaxf(sc[4], sc[5]), fmaxf(sc[6], sc[7])));
    tm = xrow_max(tm);
    __syncthreads();
#pragma unroll
    for (int i = 2; i < 4; ++i) LDKV(pk[i], pv[i], STEP_BASE(r0, i) + loff0);
    char* Pb = aux;
    float* tmax = (float*)(aux + 8192);
    float* tsum = (float*)(aux + 8192 + 512);
    if (g == 0) tmax[kp * 32 + 16 * hh + c] = tm;
    __syncthreads();
    const float Mh = fmaxf(fmaxf(tmax[16 * hh + c], tmax[32 + 16 * hh + c]), fmaxf(tmax[64 + 16 * hh + c], tmax[96 + 16 * hh + c]));
    {
      float p[8];
#pragma unroll
      for (int j = 0; j < 8; ++j) p[j] = ex2((sc[j] - Mh) * LOG2E);
      const float lsum = xrow_sum(((p[0] + p[1]) + (p[2] + p[3])) + ((p[4] + p[5]) + (p[6] + p[7])));
      if (g == 0) tsum[kp * 32 + 16 * hh + c] = lsum;
#pragma unroll
      for (int t = 0; t < 2; ++t)
        *(u32x2*)(Pb + (16 * hh + c) * 256 + 16 * ((2 * (2 * kp + t) + (g >> 1)) ^ swz(c)) + 8 * (g & 1)) = u32x2{pack2(p[4 * t], p[4 * t + 1]), pack2(p[4 * t + 2], p[4 * t + 3])};
    }
    __syncthreads();
#pragma unroll
    for (int i = 4; i < 6; ++i) LDKV(pk[i], pv[i], STEP_BASE(r0, i) + loff0);
    f32x4 acc[4][2];
#pragma unroll
    for (int i = 0; i < 4; ++i) { acc[i][0] = f32x4{0.f, 0.f, 0.f, 0.f}; acc[i][1] = f32x4{0.f, 0.f, 0.f, 0.f}; }
#pragma unroll 1
    for (int ks = 0; ks < 4; ++ks) {
      const bf16x8 b0 = *(const bf16x8*)(Pb + c * 256 + 16 * ((4 * ks + g) ^ swz(c)));
      const bf16x8 b1 = *(const bf16x8*)(Pb + (16 + c) * 256 + 16 * ((4 * ks + g) ^ swz(c)));
#pragma unroll
      for (int ct = 0; ct < 4; ++ct) {
        const bf16x8 af = tr_frag<1024>(smem, 4 * wave + ct, ks, lane);
        acc[ct][0] = mfma16(af, b0, acc[ct][0]);
        acc[ct][1] = mfma16(af, b1, acc[ct][1]);
      }
    }
    __syncthreads();
    STAMP(3);
#pragma unroll
    for (int ct = 0; ct < 4; ++ct)
#pragma unroll
      for (int ht = 0; ht < 2; ++ht)
        *(u32x2*)(smem + (16 * ht + c) * 1040 + (64 * wave + 16 * ct + 4 * g) * 2) = u32x2{pack2(acc[ct][ht][0], acc[ct][ht][1]), pack2(acc[ct][ht][2], acc[ct][ht][3])};
    __syncthreads();
#pragma unroll
    for (int i = 0; i < 4; ++i) {
      const int head = 4 * wave + i;
      const u32x4 v = *(const u32x4*)(smem + head * 1040 + 16 * lane);
      __builtin_amdgcn_raw_buffer_store_b128(v, wsr, (unsigned)(WS_APART + (((size_t)b * 32 + head) * 64 + ck) * 1024 + 16 * lane), 0, 16);
    }
    asm volatile("s_waitcnt vmcnt(0)" ::: "memory");
    __syncthreads();
    if (wave < 2 && g == 0) {
      const int head = 16 * hh + c;
      const float L = (tsum[head] + tsum[32 + head]) + (tsum[64 + head] + tsum[96 + head]);
      u64* sp = (u64*)(ws + WS_ASTAT) + (((size_t)head * 2 + b) * 64 + ck) * 2;
      st_g(sp, mk_gran(tagsh, Mh)); st_g(sp + 1, mk_gran(tagsh, L));
    }
    STAMP(4);
#pragma unroll
    for (int i = 6; i < 8; ++i) LDKV(pk[i], pv[i], STEP_BASE(r0, i) + loff0);
    pool_stream<true>(Kc, Vc, aux + POOL_OFF, 1, __builtin_amdgcn_readfirstlane(((volatile int*)(aux + POOL_OFF + PL_W))[1]), tid, pk, pv);
    STAMP(6);
  }
}

extern "C" void kernel_launch(void* const* d_in, const int* in_sizes, int n_in, void* d_out, int out_size, void* d_ws, size_t ws_size, hipStream_t stream) {
  (void)hipFuncSetAttribute((const void*)fused_kernel, hipFuncAttributeMaxDynamicSharedMemorySize, LDS_BYTES);
  fused_kernel<<<256, NTHR, LDS_BYTES, stream>>>((const u16*)d_in[0], (const u16*)d_in[1], (const u16*)d_in[2], (const u16*)d_in[3], (const u16*)d_in[4],
      (const u16*)d_in[5], (const u16*)d_in[6], (const u16*)d_in[7], (const u16*)d_in[8], (const u16*)d_in[9], (const u16*)d_in[10], (u16*)d_out, (char*)d_ws);
}
```

```cpp
#include <hip/hip_runtime.h>
#include <stdint.h>

typedef unsigned short u16;
typedef unsigned long long u64;
typedef unsigned u32x4 __attribute__((ext_vector_type(4)));
typedef unsigned u32x2 __attribute__((ext_vector_type(2)));
typedef float f32x4 __attribute__((ext_vector_type(4)));
typedef short s16x4 __attribute__((ext_vector_type(4)));
typedef __attribute__((ext_vector_type(2))) __bf16 bf16x2;
typedef __attribute__((ext_vector_type(8))) __bf16 bf16x8;
#define GAS __attribute__((address_space(1)))
#define LAS __attribute__((address_space(3)))
#define DI __device__ __forceinline__
#define RLX_AGENT __ATOMIC_RELAXED, __HIP_MEMORY_SCOPE_AGENT
#define PSLEEP 4
#define THR 5

constexpr float SCALE = 0.08838834764831845f;
constexpr float LOG2E = 1.4426950408889634f;
constexpr unsigned SPIN_CAP = 1u << 14;
constexpr int NW = 8, NTHR = 512;
constexpr int LDS_BYTES = 163840;
constexpr int POOL_OFF = 12288;

constexpr int NSLOT = 2;
constexpr size_t WS_EPOCH = 0;
constexpr size_t WS_CTR   = 1024;
constexpr size_t WS_QSEAL = 4096;
constexpr size_t WS_QABS  = 16384;
constexpr size_t WS_QB    = WS_QABS + 65536;
constexpr size_t WS_QAP   = WS_QB + 65536;
constexpr size_t WS_QBP   = WS_QAP + 524288;
constexpr size_t WS_CTXP  = WS_QBP + 524288;
constexpr size_t WS_ASTAT = WS_CTXP + 131072;
constexpr size_t WS_BPART = WS_ASTAT + 131072;
constexpr size_t WS_YA    = WS_BPART + 64 * NSLOT * 144 * 8;
constexpr size_t WS_YB    = WS_YA + 524288;
constexpr size_t WS_APART = WS_YB + 524288;
constexpr size_t WS_DBG   = WS_APART + 4194304;
constexpr size_t WS_END   = WS_DBG + 65536;

DI float bf_lo(unsigned x) { return __uint_as_float(x << 16); }
DI float bf_hi(unsigned x) { return __uint_as_float(x & 0xffff0000u); }
DI float rbf(float f) { unsigned u = __float_as_uint(f); u += 0x7fffu + ((u >> 16) & 1u); return __uint_as_float(u & 0xffff0000u); }
DI unsigned f2bf(float f) { unsigned u = __float_as_uint(f); u += 0x7fffu + ((u >> 16) & 1u); return u >> 16; }
DI unsigned pack2(float lo, float hi) { return f2bf(lo) | (f2bf(hi) << 16); }
DI float dot2(unsigned a, unsigned b, float acc) {
  return __builtin_amdgcn_fdot2_f32_bf16(__builtin_bit_cast(bf16x2, a), __builtin_bit_cast(bf16x2, b), acc, false);
}
template <int CTRL> DI float dpp(float x) {
  return __builtin_bit_cast(float, __builtin_amdgcn_mov_dpp(__builtin_bit_cast(int, x), CTRL, 0xf, 0xf, true));
}
constexpr int XOR1 = 0xB1, XOR2 = 0x4E, XOR7 = 0x141, XOR8 = 0x128;
DI float row16_sum(float x) { x += dpp<XOR8>(x); x += dpp<XOR7>(x); x += dpp<XOR2>(x); x += dpp<XOR1>(x); return x; }
DI float row16_max(float x) { x = fmaxf(x, dpp<XOR8>(x)); x = fmaxf(x, dpp<XOR7>(x)); x = fmaxf(x, dpp<XOR2>(x)); x = fmaxf(x, dpp<XOR1>(x)); return x; }
DI float xrow_max(float x) {
  auto s = __builtin_amdgcn_permlane16_swap(__float_as_uint(x), __float_as_uint(x), false, false);
  x = fmaxf(__uint_as_float(s[0]), __uint_as_float(s[1]));
  auto t = __builtin_amdgcn_permlane32_swap(__float_as_uint(x), __float_as_uint(x), false, false);
  return fmaxf(__uint_as_float(t[0]), __uint_as_float(t[1]));
}
DI float xrow_sum(float x) {
  auto s = __builtin_amdgcn_permlane16_swap(__float_as_uint(x), __float_as_uint(x), false, false);
  x = __uint_as_float(s[0]) + __uint_as_float(s[1]);
  auto t = __builtin_amdgcn_permlane32_swap(__float_as_uint(x), __float_as_uint(x), false, false);
  return __uint_as_float(t[0]) + __uint_as_float(t[1]);
}
DI float x32_sum(float x) {
  auto t = __builtin_amdgcn_permlane32_swap(__float_as_uint(x), __float_as_uint(x), false, false);
  return __uint_as_float(t[0]) + __uint_as_float(t[1]);
}
template <int CTRL> DI unsigned dppu(unsigned x) { return (unsigned)__builtin_amdgcn_mov_dpp((int)x, CTRL, 0xf, 0xf, true); }
DI unsigned row16_sumu(unsigned x) { x += dppu<XOR8>(x); x += dppu<XOR7>(x); x += dppu<XOR2>(x); x += dppu<XOR1>(x); return x; }
DI unsigned x16_sumu(unsigned x) { auto s = __builtin_amdgcn_permlane16_swap(x, x, false, false); return s[0] + s[1]; }
DI unsigned x32_sumu(unsigned x) { auto s = __builtin_amdgcn_permlane32_swap(x, x, false, false); return s[0] + s[1]; }
DI unsigned call_key(u64 tagsh) { unsigned k = (unsigned)(tagsh >> 24) * 0x9E3779B1u; k ^= k >> 15; return k * 0x85EBCA77u + 0x6A09E667u; }
DI unsigned cs_fold(unsigned s) { return (s ^ (s >> 24) * 0x9E37u) & 0xFFFFFFu; }
DI u32x4 ldnt(const void* p) { return __builtin_nontemporal_load((const u32x4*)p); }
DI u64 ld_g(const void* p) { return __hip_atomic_load((GAS u64*)p, RLX_AGENT); }
DI void st_g(void* p, u64 v) { __hip_atomic_store((GAS u64*)p, v, RLX_AGENT); }
DI u64 mk_gran(u64 tagsh, float v) { unsigned u = __float_as_uint(v); u += 0x7fu + ((u >> 8) & 1u); return tagsh | (u64)(u >> 8); }
DI bool gran_ok(u64 g, u64 tagsh) { return (g & 0xFFFFFFFFFF000000ull) == tagsh; }
DI float gran_val(u64 g) { return __uint_as_float(((unsigned)g & 0xFFFFFFu) << 8); }
DI int swz(int row) { return ((row & 3) << 1) | (row & 8); }
DI float score_round(float d) { return rbf(rbf(d) * SCALE); }
DI float ex2(float x) { return __builtin_amdgcn_exp2f(x); }
DI void st8(float* p, const float (&a)[8]) { *(f32x4*)p = f32x4{a[0], a[1], a[2], a[3]}; *(f32x4*)(p + 4) = f32x4{a[4], a[5], a[6], a[7]}; }
DI void dma16(const void* g, char* l) {
  unsigned keep; const unsigned dst = __builtin_amdgcn_readfirstlane((unsigned)(size_t)(LAS char*)l);
  asm volatile("s_mov_b32 %0, m0\n\ts_mov_b32 m0, %2\n\ts_nop 0\n\tglobal_load_lds_dwordx4 %1, off nt\n\ts_mov_b32 m0, %0" : "=&s"(keep) : "v"(g), "s"(dst) : "memory");
}
#define VMWAIT0() asm volatile("s_waitcnt vmcnt(0)" ::: "memory")
DI f32x4 mfma16(bf16x8 a, bf16x8 b, f32x4 c) { return __builtin_amdgcn_mfma_f32_16x16x32_bf16(a, b, c, 0, 0, 0); }

#define STAMP(i) do {} while (0)

template <int ROWB> DI bf16x8 tr_frag(const char* img, int nt, int ks, int lane) {
  const int g = lane >> 4, q4 = (lane & 15) >> 2, p4 = lane & 3;
  const int row = 32 * ks + 8 * g + q4, ch = 2 * nt + (p4 >> 1);
  const char* p = img + row * ROWB + 16 * (ch ^ swz(row)) + 8 * (p4 & 1);
  const s16x4 lo = __builtin_amdgcn_ds_read_tr16_b64_v4i16((LAS s16x4*)p);
  const s16x4 hi = __builtin_amdgcn_ds_read_tr16_b64_v4i16((LAS s16x4*)(p + 4 * ROWB));
  return __builtin_bit_cast(bf16x8, __builtin_shufflevector(lo, hi, 0, 1, 2, 3, 4, 5, 6, 7));
}
template <int NC> DI bf16x8 x_frag(const char* xf, int stride, int ks, int lane) {
  const int g = lane >> 4, c = lane & 15;
  u32x4 v = {0u, 0u, 0u, 0u};
  if (c < NC) v = *(const u32x4*)(xf + c * stride + 64 * ks + 16 * g);
  return __builtin_bit_cast(bf16x8, v);
}
#define STR2(x) #x
#define STR(x) STR2(x)
#define THROTTLE() asm volatile("s_waitcnt vmcnt(" STR(THR) ")" ::: "memory")
#define DTHR THR
#define DTHROTTLE() asm volatile("s_waitcnt vmcnt(" STR(DTHR) ")" ::: "memory")
template <bool TH = true> DI void dma_rows256(const char* gbase, size_t gstride, char* img, int row0, int n, int lane) {
  const int rr = lane >> 4, pos = lane & 15;
#pragma unroll 4
  for (int i = 0; i < n; ++i) { const int row = row0 + 4 * i + rr; if (TH) DTHROTTLE(); dma16(gbase + (size_t)row * gstride + 16 * (pos ^ swz(row)), img + (row0 + 4 * i) * 256); }
}
template <bool TH = true> DI void dma_rows1k(const char* gbase, char* img, int row0, int n, int lane) {
#pragma unroll 4
  for (int i = 0; i < n; ++i) { const int row = row0 + i; if (TH) DTHROTTLE(); dma16(gbase + (size_t)row * 1024 + 16 * (lane ^ swz(row)), img + row * 1024); }
}

struct BState { float m, l; float acc[8]; };
DI unsigned cvtpk(float lo, float hi) { typedef __attribute__((ext_vector_type(2))) float f2; typedef __attribute__((ext_vector_type(2))) __bf16 b2;
  return __builtin_bit_cast(unsigned, __builtin_convertvector(f2{lo, hi}, b2)); }
DI void b_step2(const u32x4& k0, const u32x4& v0, const u32x4& k1, const u32x4& v1, const u32x4& q, BState& st) {
  float d0 = dot2(k0[0], q[0], 0.f), d1 = dot2(k1[0], q[0], 0.f);
#pragma unroll
  for (int j = 1; j < 4; ++j) { d0 = dot2(k0[j], q[j], d0); d1 = dot2(k1[j], q[j], d1); }
  d0 = row16_sum(d0); d1 = row16_sum(d1);
  unsigned r = cvtpk(d0, d1);
  r = cvtpk(bf_lo(r) * SCALE, bf_hi(r) * SCALE);
  const float s0 = bf_lo(r), s1 = bf_hi(r), sm = fmaxf(s0, s1);
  if (__any(sm > st.m)) {
    const float mn = fmaxf(st.m, sm);
    const float alpha = ex2((st.m - mn) * LOG2E);
    st.m = mn; st.l *= alpha;
#pragma unroll
    for (int j = 0; j < 8; ++j) st.acc[j] *= alpha;
  }
  const float ml = st.m * LOG2E;
  const float p0 = ex2(fmaf(s0, LOG2E, -ml)), p1 = ex2(fmaf(s1, LOG2E, -ml));
  st.l += p0 + p1;
#pragma unroll
  for (int j = 0; j < 4; ++j) {
    st.acc[2 * j] = fmaf(p1, bf_lo(v1[j]), fmaf(p0, bf_lo(v0[j]), st.acc[2 * j]));
    st.acc[2 * j + 1] = fmaf(p1, bf_hi(v1[j]), fmaf(p0, bf_hi(v0[j]), st.acc[2 * j + 1]));
  }
}
#define RAWBAR() do { asm volatile("s_waitcnt lgkmcnt(0)" ::: "memory"); __builtin_amdgcn_s_barrier(); asm volatile("" ::: "memory"); } while (0)
#define RD 4
#define PU0 8
#define PU1 8
#define LDKV(K_, V_, OFF) asm volatile("global_load_dwordx4 %0, %2, %3 nt\n\tglobal_load_dwordx4 %1, %2, %4 nt" : "=&v"(K_), "=&v"(V_) : "v"(OFF), "s"(Kc), "s"(Vc) : "memory")
#define WAITKV2(K0, V0, K1, V1, N) asm volatile("s_waitcnt vmcnt(%4)" : "+v"(K0), "+v"(V0), "+v"(K1), "+v"(V1) : "i"(N) : "memory")
DI void kv_stream(const u16* Kc, const u16* Vc, char* ws, const __amdgpu_buffer_rsrc_t& wsr, char* lds  , int pair, int u0, int nu, int slot, u64 tagsh, int tid) {
  const int lane = tid & 63, wave = __builtin_amdgcn_readfirstlane(tid >> 6), g = lane >> 4, c = lane & 15;
  const unsigned base = (unsigned)pair * 2097152u + (unsigned)((6144 + 128 * u0 + 16 * wave + g) * 256 + 16 * c);
#define STEP_OFF(S) ((unsigned)(((S) >> 2) * 32768 + ((S) & 3) * 1024))
  unsigned* qs = (unsigned*)(lds + 8 * 528);
  u32x4 kr[RD], vr[RD];
#pragma unroll
  for (int i = 0; i < RD; ++i) LDKV(kr[i], vr[i], base + STEP_OFF(i));
  if (wave == 0) {
    const unsigned qo = (unsigned)WS_QBP + (unsigned)((pair & 31) * 2048 + (pair >> 5) * 128 + 2 * lane) * 8u;
    const unsigned th = (unsigned)(tagsh >> 32), tl = (unsigned)tagsh;
    float s0, s1;
#pragma unroll 1
    for (unsigned spins = 0; spins < SPIN_CAP; ++spins) {
      asm volatile("" ::: "memory");
      u32x4 v[8]; bool ok = true; s0 = 0.f; s1 = 0.f;
#pragma unroll
      for (int k = 0; k < 8; ++k) v[k] = __builtin_amdgcn_raw_buffer_load_b128(wsr, qo + 2048u * k, 0, 16);
#pragma unroll
      for (int k = 0; k < 8; ++k) {
        ok = ok && v[k][1] == th && (v[k][0] & 0xFF000000u) == tl && v[k][3] == th && (v[k][2] & 0xFF000000u) == tl;
        s0 += __uint_as_float(v[k][0] << 8); s1 += __uint_as_float(v[k][2] << 8);
      }
      if (__all(ok)) break;
      __builtin_amdgcn_s_sleep(2);
    }
    qs[lane] = pack2(s0, s1);
  }
  RAWBAR();
  const u32x4 q = *(const u32x4*)(qs + 4 * c);
  BState st; st.m = -INFINITY; st.l = 0.f;
#pragma unroll
  for (int j = 0; j < 8; ++j) st.acc[j] = 0.f;
  const int ns = 4 * nu;
  int s = 0;
#pragma unroll 1
  for (; s + RD < ns; s += RD) {
#pragma unroll
    for (int i = 0; i < RD; i += 2) {
      WAITKV2(kr[i], vr[i], kr[i + 1], vr[i + 1], 2 * (RD - 2));
      b_step2(kr[i], vr[i], kr[i + 1], vr[i + 1], q, st);
      __builtin_amdgcn_sched_barrier(0);
      const unsigned o = base + STEP_OFF(s + i + RD);
      LDKV(kr[i], vr[i], o);
      LDKV(kr[i + 1], vr[i + 1], o + 1024u);
    }
  }
#pragma unroll
  for (int i = 0; i < RD; i += 2) {
    WAITKV2(kr[i], vr[i], kr[i + 1], vr[i + 1], 2 * (RD - 2 - i));
    b_step2(kr[i], vr[i], kr[i + 1], vr[i + 1], q, st);
  }
  {
    const float Mw = xrow_max(st.m);
    const float f = ex2((st.m - Mw) * LOG2E);
#pragma unroll
    for (int j = 0; j < 8; ++j) st.acc[j] = xrow_sum(st.acc[j] * f);
    const float l = xrow_sum(st.l * f);
    float* rec = (float*)lds + wave * 132;
    if (g == 0) { st8(rec + 4 + 8 * c, st.acc); if (c == 0) { rec[0] = Mw; rec[1] = l; } }
  }
  RAWBAR();
  if (tid < 128) {
    const float* recs = (const float*)lds;
    float M = recs[0];
#pragma unroll
    for (int w = 1; w < NW; ++w) M = fmaxf(M, recs[w * 132]);
    float o = 0.f, L = 0.f;
#pragma unroll
    for (int w = 0; w < NW; ++w) { const float wt = ex2((recs[w * 132] - M) * LOG2E); o = fmaf(wt, recs[w * 132 + 4 + tid], o); L = fmaf(wt, recs[w * 132 + 1], L); }
    u64* sl = (u64*)(ws + WS_BPART) + ((size_t)pair * NSLOT + slot) * 144;
    st_g(sl + tid, mk_gran(tagsh, o));
    if (tid < 2) st_g(sl + 128 + tid, mk_gran(tagsh, tid ? L : M));
  }
}

DI void stage1_compute(const char* img, const char* xf, u64* dst, u64 tagsh, int tid) {
  const int lane = tid & 63, wave = tid >> 6;
  const bf16x8 a0 = tr_frag<256>(img, wave, 0, lane), a1 = tr_frag<256>(img, wave, 1, lane), x0 = x_frag<2>(xf, 128, 0, lane), x1 = x_frag<2>(xf, 128, 1, lane);
  __builtin_amdgcn_sched_barrier(0);
  f32x4 d = mfma16(a0, x0, f32x4{0.f, 0.f, 0.f, 0.f});
  d = mfma16(a1, x1, d);
  const int b = (lane >> 4) & 1, n = lane & 15, srcl = 16 * (n >> 2) + b;
  float v = 0.f;
#pragma unroll
  for (int r = 0; r < 4; ++r) { const float t0 = __shfl(d[r], srcl); if ((n & 3) == r) v = t0; }
  if (lane < 32) st_g(dst + b * 128 + 16 * wave + n, mk_gran(tagsh, v));
}

DI void out_stage(const float* ybuf, float* red, size_t yoff, const __amdgpu_buffer_rsrc_t& wsr, u16* out, int srv, int colbase, u64 tagsh, int tid) {
  const int lane = tid & 63, wave = tid >> 6;
  {
    const u64 ga = mk_gran(tagsh, ybuf[2 * tid]), gb = mk_gran(tagsh, ybuf[2 * tid + 1]);
    __builtin_amdgcn_raw_buffer_store_b128(u32x4{(unsigned)ga, (unsigned)(ga >> 32), (unsigned)gb, (unsigned)(gb >> 32)}, wsr, (unsigned)(yoff + (size_t)srv * 8192 + 16 * tid), 0, 16);
  }
  const int b = srv >> 5, m0 = (srv & 31) * 16;
  const unsigned off = (unsigned)(yoff + (size_t)(tid >> 3) * 8192 + (b * 512 + m0 + 2 * (tid & 7)) * 8);
  const unsigned th = (unsigned)(tagsh >> 32), tl = (unsigned)tagsh;
  u32x4 v; unsigned spins = 0;
#pragma unroll 1
  for (;;) {
    asm volatile("" ::: "memory");
    v = __builtin_amdgcn_raw_buffer_load_b128(wsr, off, 0, 16);
    if (__all(v[1] == th && (v[0] & 0xFF000000u) == tl && v[3] == th && (v[2] & 0xFF000000u) == tl)) break;
    if (++spins > SPIN_CAP) break;
    __builtin_amdgcn_s_sleep(1);
  }
  float f0 = __uint_as_float(v[0] << 8), f1 = __uint_as_float(v[2] << 8);
  f0 += dpp<XOR8>(f0); f1 += dpp<XOR8>(f1);
  f0 = xrow_sum(f0); f1 = xrow_sum(f1);
  if (lane < 8) { red[wave * 16 + 2 * lane] = f0; red[wave * 16 + 2 * lane + 1] = f1; }
  __syncthreads();
  if (tid < 16) {
    float s = 0.f;
#pragma unroll
    for (int w = 0; w < NW; ++w) s += red[w * 16 + tid];
    out[b * 1024 + colbase + m0 + tid] = (u16)f2bf(s);
  }
}

__global__ void __launch_bounds__(NTHR) fused_kernel(
    const u16* __restrict__ Ax, const u16* __restrict__ ckv, const u16* __restrict__ Awq, const u16* __restrict__ Awuk,
    const u16* __restrict__ Awuv, const u16* __restrict__ Awo, const u16* __restrict__ Bx, const u16* __restrict__ Kc,
    const u16* __restrict__ Vc, const u16* __restrict__ Bwq, const u16* __restrict__ Bwo, u16* __restrict__ out, char* __restrict__ ws) {
  extern __shared__ __attribute__((aligned(16))) char smem[];
  const int tid = threadIdx.x, lane = tid & 63, wave = __builtin_amdgcn_readfirstlane(tid >> 6);
  const int g = lane >> 4, c = lane & 15;
  const int bid = blockIdx.x, xg = bid & 7, idx = bid >> 3;
  asm volatile("" :: "s"(Ax), "s"(ckv), "s"(Awq), "s"(Awuk), "s"(Awuv), "s"(Awo), "s"(Bx), "s"(Kc), "s"(Vc), "s"(Bwq), "s"(Bwo), "s"(out), "s"(ws));
  STAMP(0);
#define GET_TAG() u64 epoch; asm volatile("s_load_dwordx2 %0, %1, 0x0\n\ts_waitcnt lgkmcnt(0)" : "=&s"(epoch) : "s"(ws) : "memory"); const u64 tagsh = (epoch + 1) << 24
  const __amdgpu_buffer_rsrc_t wsr = __builtin_amdgcn_make_buffer_rsrc(ws, 0, (int)WS_END, 0x00020000);
  char* aux = smem + 131072;
  char* s1img = aux;
  char* s1x = smem + 131072 - 512;
  const int h1 = 4 * xg + (idx >> 3), j1 = idx & 7;
  if (tid < 32) dma16((tid < 16 ? Ax : Bx) + ((tid >> 3) & 1) * 512 + 64 * j1 + 8 * (tid & 7), s1x);
  dma_rows256<false>((const char*)(Awq + ((size_t)64 * j1 * 32 + h1) * 128), 8192, s1img, 8 * wave, 2, lane);
  dma_rows256<false>((const char*)(Bwq + (size_t)64 * j1 * 4096 + h1 * 128), 8192, s1img + 16384, 8 * wave, 2, lane);
#define S1_WAIT(N) do { asm volatile("s_waitcnt vmcnt(%0) lgkmcnt(0)" :: "i"(N) : "memory"); __builtin_amdgcn_s_barrier(); asm volatile("" ::: "memory"); } while (0)
#define STAGE1(NL) do { S1_WAIT((NL) + 2); stage1_compute(s1img, s1x, (u64*)(ws + WS_QAP) + ((size_t)h1 * 8 + j1) * 256, tagsh, tid); \
    S1_WAIT((NL) + 1); stage1_compute(s1img + 16384, s1x + 256, (u64*)(ws + WS_QBP) + ((size_t)h1 * 8 + j1) * 256, tagsh, tid); RAWBAR(); } while (0)

  if (idx < 8) {
    const int h = 4 * xg + (idx >> 1), half = idx & 1, srv = h * 2 + half;
    char* xf = aux;
    char* qf = aux + 4224;
    char* xf2 = aux + 18432;
    float* rec = (float*)(aux + 5376);
    float* ybuf = (float*)(aux + 13824);
    float* red = (float*)(aux + 17920);
    dma_rows256<false>((const char*)(Awuk + ((size_t)256 * half * 32 + h) * 128), 8192, smem, 32 * wave, 8, lane);
    GET_TAG();
    const unsigned key = call_key(tagsh);
    STAGE1(8);
    VMWAIT0();
    STAMP(8);
    bf16x8 wkf[2][4];
#pragma unroll
    for (int t = 0; t < 2; ++t)
#pragma unroll
      for (int s = 0; s < 4; ++s) wkf[t][s] = *(const bf16x8*)(smem + (32 * wave + 16 * t + c) * 256 + 16 * ((4 * s + g) ^ swz(c)));
#define SUM8(QP, S) do { u64 gq[8]; unsigned spins = 0; \
      for (;;) { bool ok = true; _Pragma("unroll") for (int k = 0; k < 8; ++k) gq[k] = ld_g((QP) + k * 256); \
        _Pragma("unroll") for (int k = 0; k < 8; ++k) ok = ok && gran_ok(gq[k], tagsh); \
        if (__all(ok)) break; if (++spins > SPIN_CAP) break; __builtin_amdgcn_s_sleep(1); } \
      S = 0.f; _Pragma("unroll") for (int k = 0; k < 8; ++k) S += gran_val(gq[k]); } while (0)
    if (wave < 4) {
      float s; SUM8((const u64*)(ws + WS_QAP) + (size_t)h * 2048 + tid, s);
      *(u16*)(qf + (tid >> 7) * 272 + 2 * (tid & 127)) = (u16)f2bf(s);
    }
    __syncthreads();
    STAMP(10);
    {
      bf16x8 qb[4];
#pragma unroll
      for (int s = 0; s < 4; ++s) qb[s] = x_frag<2>(qf, 272, s, lane);
      __builtin_amdgcn_sched_barrier(0);
      unsigned cs = 0;
#pragma unroll
      for (int t = 0; t < 2; ++t) {
        f32x4 d = {0.f, 0.f, 0.f, 0.f};
#pragma unroll
        for (int s = 0; s < 4; ++s) d = mfma16(wkf[t][s], qb[s], d);
        const unsigned w0 = pack2(d[0], d[1]) ^ key, w1 = pack2(d[2], d[3]) ^ key;
        cs += w0 + w1;
        if (c < 2) {
          const size_t off = WS_QABS + ((((size_t)c * 2 + (h >> 4)) * 16 + 8 * half + wave) * 64 + (2 * t + (g >> 1)) * 16 + (h & 15)) * 16 + 8 * (g & 1);
          st_g(ws + off, (u64)w0 | ((u64)w1 << 32));
        }
      }
      cs = x32_sumu(x16_sumu(cs));
      if (lane < 2) st_g((u64*)(ws + WS_QSEAL) + (((size_t)lane * 2 + (h >> 4)) * 16 + 8 * half + wave) * 16 + (h & 15), tagsh | (u64)cs_fold(cs));
    }
    STAMP(1);
    VMWAIT0();
    RAWBAR();
    dma_rows1k<false>((const char*)(Awo + (size_t)h * 128 * 512), smem, 16 * wave, 16, lane);
    S1_WAIT(0);
    bf16x8 wof[4][4];
#pragma unroll
    for (int t = 0; t < 4; ++t)
#pragma unroll
      for (int k = 0; k < 4; ++k) wof[t][k] = tr_frag<1024>(smem, 4 * wave + t, k, lane);
    RAWBAR();
    dma_rows256<false>((const char*)(Awuv + ((size_t)256 * half * 32 + h) * 128), 8192, smem, 32 * wave, 8, lane);
    STAMP(11);
    {
      const int b = wave >> 2, ck0 = 8 * (wave & 3);
      const u64* sp = (const u64*)(ws + WS_ASTAT) + (((size_t)h * 2 + b) * 32 + ck0) * 4 + (lane & 31);
      u64 gs; unsigned spins = 0;
#pragma unroll 1
      for (;;) { gs = ld_g(sp); if (__any(gran_ok(gs, tagsh))) break; if (++spins > SPIN_CAP) break; __builtin_amdgcn_s_sleep(PSLEEP); }
      STAMP(9);
      const int hi = lane >> 5, l5 = lane & 31;
      u32x4 pr[4];
#pragma unroll 1
      for (;;) {
        asm volatile("" ::: "memory");
        gs = ld_g(sp);
#pragma unroll
        for (int i = 0; i < 4; ++i)
          pr[i] = __builtin_amdgcn_raw_buffer_load_b128(wsr, (unsigned)(WS_APART + ((((size_t)b * 32 + h) * 32 + ck0 + 2 * i + hi) * 512 + 256 * half + 8 * l5) * 2), 0, 16);
        bool ok = gran_ok(gs, tagsh);
#pragma unroll
        for (int i = 0; i < 4; ++i) {
          const unsigned cs = cs_fold(x16_sumu(row16_sumu((pr[i][0] + pr[i][1]) + (pr[i][2] + pr[i][3]))));
          const unsigned elo = (unsigned)__builtin_amdgcn_readlane((int)(unsigned)gs, 8 * i + 2 + half) & 0xFFFFFFu, ehi = (unsigned)__builtin_amdgcn_readlane((int)(unsigned)gs, 8 * i + 6 + half) & 0xFFFFFFu;
          ok = ok && cs == (hi ? ehi : elo);
        }
        if (__all(ok)) break;
        if (++spins > SPIN_CAP) break;
      }
      const float sv = gran_val(gs);
      const float Mw = xrow_max(row16_max((lane & 3) ? -INFINITY : sv));
      const float we = ex2((sv - Mw) * LOG2E);
      const float wl = dpp<XOR1>(we) * sv;
      const float Lw = xrow_sum(row16_sum(((lane & 3) == 1 && lane < 32) ? wl : 0.f));
      float a[8];
#pragma unroll
      for (int j = 0; j < 8; ++j) a[j] = 0.f;
#pragma unroll
      for (int i = 0; i < 4; ++i) {
        const float wlo = __builtin_bit_cast(float, __builtin_amdgcn_readlane(__builtin_bit_cast(int, we), 8 * i));
        const float whi = __builtin_bit_cast(float, __builtin_amdgcn_readlane(__builtin_bit_cast(int, we), 8 * i + 4));
        const float wt = hi ? whi : wlo;
#pragma unroll
        for (int j = 0; j < 4; ++j) { const unsigned x = pr[i][j] ^ key; a[2 * j] = fmaf(wt, bf_lo(x), a[2 * j]); a[2 * j + 1] = fmaf(wt, bf_hi(x), a[2 * j + 1]); }
      }
#pragma unroll
      for (int j = 0; j < 8; ++j) a[j] = x32_sum(a[j]);
      float* rc = rec + wave * 260;
      if (hi == 0) st8(rc + 4 + 8 * l5, a);
      if (lane == 0) { rc[0] = Mw; rc[1] = Lw; }
    }
    VMWAIT0();
    __syncthreads();
    STAMP(2);
    bf16x8 wvf[8];
#pragma unroll
    for (int ks = 0; ks < 8; ++ks) wvf[ks] = tr_frag<256>(smem, wave, ks, lane);
    {
      const int bb = tid >> 8, c2 = tid & 255;
      const float* r0 = rec + (bb * 4) * 260;
      const float M = fmaxf(fmaxf(r0[0], r0[260]), fmaxf(r0[520], r0[780]));
      float o = 0.f, L = 0.f;
#pragma unroll
      for (int w = 0; w < 4; ++w) { const float ew = ex2((r0[w * 260] - M) * LOG2E); o = fmaf(ew, r0[w * 260 + 4 + c2], o); L = fmaf(ew, r0[w * 260 + 1], L); }
      const float v = o / L;
      const float vh = rbf(v);
      const int cr = (c2 & 0xE3) | ((c2 & 4) << 2) | ((c2 & 0x18) >> 1);
      *(u16*)(xf + bb * 1040 + 2 * cr) = (u16)(__float_as_uint(vh) >> 16);
      *(u16*)(xf + (2 + bb) * 1040 + 2 * cr) = (u16)f2bf(v - vh);
    }
    __syncthreads();
    {
      f32x4 d = {0.f, 0.f, 0.f, 0.f};
      bf16x8 xb8[8];
#pragma unroll
      for (int ks = 0; ks < 8; ++ks) xb8[ks] = x_frag<4>(xf, 1040, ks, lane);
      __builtin_amdgcn_sched_barrier(0);
#pragma unroll
      for (int ks = 0; ks < 8; ++ks) d = mfma16(wvf[ks], xb8[ks], d);
#pragma unroll
      for (int j = 0; j < 4; ++j) d[j] += dpp<XOR2>(d[j]);
      if (c < 2) {
        const int dk = (16 * wave + 4 * g) * 2;
        const float h0 = rbf(d[0]), h1_ = rbf(d[1]), h2 = rbf(d[2]), h3 = rbf(d[3]);
        *(u32x2*)(xf2 + c * 272 + dk) = u32x2{pack2(h0, h1_), pack2(h2, h3)};
        *(u32x2*)(xf2 + (2 + c) * 272 + dk) = u32x2{pack2(d[0] - h0, d[1] - h1_), pack2(d[2] - h2, d[3] - h3)};
      }
    }
    __syncthreads();
    STAMP(3);
    {
      bf16x8 xb[4];
#pragma unroll
      for (int k = 0; k < 4; ++k) xb[k] = x_frag<4>(xf2, 272, k, lane);
#pragma unroll
      for (int t = 0; t < 4; ++t) {
        f32x4 d = {0.f, 0.f, 0.f, 0.f};
#pragma unroll
        for (int k = 0; k < 4; ++k) d = mfma16(wof[t][k], xb[k], d);
#pragma unroll
        for (int j = 0; j < 4; ++j) d[j] += dpp<XOR2>(d[j]);
        if (c < 2) *(f32x4*)(ybuf + c * 512 + 16 * (4 * wave + t) + 4 * g) = d;
      }
    }
    __syncthreads();
    out_stage(ybuf, red, WS_YA, wsr, out, srv, 0, tagsh, tid);
    STAMP(4);
    if (srv == 0 && tid == 0) st_g(ws + WS_EPOCH, epoch + 1);
    STAMP(7);
  } else if (idx < 24) {
    const int p16 = idx - 8, pair = ((p16 >> 1) & 1) * 32 + 4 * xg + (p16 >> 2), part = p16 & 1;
    const int h = 4 * xg + (p16 >> 2), half = (p16 >> 1) & 1, srv = h * 2 + half;
    GET_TAG();
    STAGE1(0);
    STAMP(1);
    if (part == 0) dma_rows1k<false>((const char*)(Bwo + ((size_t)h * 128 + 64 * half) * 512), smem + 65536, 8 * wave, 8, lane);
    kv_stream(Kc, Vc, ws, wsr, smem, pair, part ? PU0 : 0, part ? PU1 : PU0, part, tagsh, tid);
    STAMP(3);
    if (part == 0) {
    char* xf2 = aux + 18432;
    float* ybuf = (float*)(aux + 13824);
    float* red = (float*)(aux + 17920);
    bf16x8 wof[4][2];
#pragma unroll
    for (int t = 0; t < 4; ++t) { wof[t][0] = tr_frag<1024>(smem + 65536, 4 * wave + t, 0, lane); wof[t][1] = tr_frag<1024>(smem + 65536, 4 * wave + t, 1, lane); }
    if (wave < 2) {
      const int b = wave;
      const u64* base = (const u64*)(ws + WS_BPART) + (size_t)(b * 32 + h) * NSLOT * 144;
      u64 go[NSLOT], gs; unsigned spins = 0;
      const int sl = min(lane >> 1, NSLOT - 1);
#pragma unroll 1
      for (;;) {
        gs = ld_g(base + sl * 144 + 128 + (lane & 1));
#pragma unroll
        for (int s = 0; s < NSLOT; ++s) go[s] = ld_g(base + s * 144 + 64 * half + lane);
        bool ok = gran_ok(gs, tagsh);
#pragma unroll
        for (int s = 0; s < NSLOT; ++s) ok = ok && gran_ok(go[s], tagsh);
        if (__all(ok)) break;
        if (++spins > SPIN_CAP) break;
        __builtin_amdgcn_s_sleep(1);
      }
      const float sv = gran_val(gs);
      const float M = xrow_max(row16_max((lane & 1) ? -INFINITY : sv));
      const float we = ex2((sv - M) * LOG2E);
      const float wl = dpp<XOR1>(we) * sv;
      const float L = xrow_sum(row16_sum(((lane & 1) && lane < 2 * NSLOT) ? wl : 0.f));
      float o = 0.f;
#pragma unroll
      for (int s = 0; s < NSLOT; ++s) o = fmaf(__builtin_bit_cast(float, __builtin_amdgcn_readlane(__builtin_bit_cast(int, we), 2 * s)), gran_val(go[s]), o);
      o /= L;
      *(u16*)(xf2 + b * 272 + 2 * lane) = (u16)f2bf(o);
    }
    __syncthreads();
    STAMP(5);
    {
      const bf16x8 b0 = x_frag<2>(xf2, 272, 0, lane), b1 = x_frag<2>(xf2, 272, 1, lane);
#pragma unroll
      for (int t = 0; t < 4; ++t) {
        f32x4 d = mfma16(wof[t][0], b0, f32x4{0.f, 0.f, 0.f, 0.f}); d = mfma16(wof[t][1], b1, d);
        if (c < 2) *(f32x4*)(ybuf + c * 512 + 16 * (4 * wave + t) + 4 * g) = d;
      }
    }
    __syncthreads();
    out_stage(ybuf, red, WS_YB, wsr, out, srv, 512, tagsh, tid);
    STAMP(7);
    }
  } else {
    const int a8 = idx - 24, a = xg * 8 + a8, b = a >> 5, ck = a & 31;
    const char* crow = (const char*)(ckv + ((size_t)b * 8192 + ck * 256) * 512);
    dma_rows1k<false>(crow, smem, 16 * wave, 2, lane);
    GET_TAG();
    STAGE1(2);
    dma_rows1k(crow, smem, 16 * wave + 2, 14, lane);
    STAMP(7);
    const unsigned key = call_key(tagsh);
    {
      const u64* sp = (const u64*)(ws + WS_QSEAL) + (size_t)b * 512 + 64 * wave + lane;
      unsigned spins = 0;
      u32x4 t[4];
#pragma unroll 1
      for (;;) {
        asm volatile("" ::: "memory");
        const u64 gs = ld_g(sp);
#pragma unroll
        for (int i = 0; i < 4; ++i) t[i] = __builtin_amdgcn_raw_buffer_load_b128(wsr, (unsigned)(WS_QABS + b * 32768 + (4 * wave + i) * 1024 + 16 * lane), 0, 16);
        bool ok = gran_ok(gs, tagsh);
#pragma unroll
        for (int i = 0; i < 4; ++i) {
          const unsigned cs = cs_fold(x32_sumu(x16_sumu((t[i][0] + t[i][1]) + (t[i][2] + t[i][3]))));
          const unsigned want = (unsigned)__shfl((int)(unsigned)gs, 16 * i + c) & 0xFFFFFFu;
          ok = ok && cs == want;
        }
        if (__all(ok)) break;
        if (++spins > SPIN_CAP) break;
        __builtin_amdgcn_s_sleep(1);
      }
      STAMP(1);
#pragma unroll
      for (int i = 0; i < 4; ++i) *(u32x4*)(aux + (4 * wave + i) * 1024 + 16 * lane) = u32x4{t[i][0] ^ key, t[i][1] ^ key, t[i][2] ^ key, t[i][3] ^ key};
    }
    u32x4 stg[16];
    const unsigned soff = (unsigned)((128 + 16 * wave) * 1024 + 16 * lane);
#define LDSTG(I) asm volatile("global_load_dwordx4 %0, %1, %2 offset:%3 nt" : "=v"(stg[I]) : "v"(soff + ((I) >> 2) * 4096u), "s"(crow), "i"(((I) & 3) * 1024) : "memory")
    LDSTG(0); LDSTG(1); LDSTG(2); LDSTG(3); LDSTG(4); LDSTG(5); LDSTG(6); LDSTG(7);
    __syncthreads();
    const int kp = wave >> 1, hh = wave & 1;
    bf16x8 qf[16];
#pragma unroll
    for (int s = 0; s < 16; ++s) qf[s] = *(const bf16x8*)(aux + (hh * 16 + s) * 1024 + 16 * lane);
    __syncthreads();
    STAMP(2);

    char* Pb = aux;
    float* tmax = (float*)(aux + 8192);
    float* tsum = (float*)(aux + 8192 + 512);
    unsigned* csum = (unsigned*)(aux + 8192 + 1024);
    if (tid < 64) csum[tid] = 0u;
    f32x4 acc[4][2];
#pragma unroll
    for (int i = 0; i < 4; ++i) { acc[i][0] = f32x4{0.f, 0.f, 0.f, 0.f}; acc[i][1] = f32x4{0.f, 0.f, 0.f, 0.f}; }
    float Mr0 = -INFINITY, Mr1 = -INFINITY, lsum = 0.f;
    {
      f32x4 s0 = {0.f, 0.f, 0.f, 0.f}, s1 = {0.f, 0.f, 0.f, 0.f};
      {
        const int f = swz(c);
        const char* rb = smem + (32 * kp + c) * 1024;
        bf16x8 fa[2][4];
#define S_LOAD(B, H) do { _Pragma("unroll") for (int j = 0; j < 2; ++j) { const int s_ = 2 * (H) + j; \
          fa[B][2 * j] = *(const bf16x8*)(rb + 256 * (s_ >> 2) + 16 * ((4 * (s_ & 3) + g) ^ f)); fa[B][2 * j + 1] = *(const bf16x8*)(rb + 16384 + 256 * (s_ >> 2) + 16 * ((4 * (s_ & 3) + g) ^ f)); } } while (0)
        S_LOAD(0, 0);
#pragma unroll
        for (int hb = 0; hb < 8; ++hb) {
          if (hb < 7) S_LOAD((hb + 1) & 1, hb + 1);
          __builtin_amdgcn_sched_barrier(0);
#pragma unroll
          for (int j = 0; j < 2; ++j) { s0 = mfma16(fa[hb & 1][2 * j], qf[2 * hb + j], s0); s1 = mfma16(fa[hb & 1][2 * j + 1], qf[2 * hb + j], s1); }
          __builtin_amdgcn_sched_barrier(0);
        }
      }
      LDSTG(8); LDSTG(9); LDSTG(10); LDSTG(11); LDSTG(12); LDSTG(13); LDSTG(14); LDSTG(15);
      float sc[8];
#pragma unroll
      for (int j = 0; j < 4; ++j) { sc[j] = score_round(s0[j]); sc[4 + j] = score_round(s1[j]); }
      float tm = fmaxf(fmaxf(fmaxf(sc[0], sc[1]), fmaxf(sc[2], sc[3])), fmaxf(fmaxf(sc[4], sc[5]), fmaxf(sc[6], sc[7])));
      tm = xrow_max(tm);
      if (g == 0) tmax[kp * 32 + 16 * hh + c] = tm;
      __syncthreads();
      const float Mn0 = fmaxf(Mr0, fmaxf(fmaxf(tmax[c], tmax[32 + c]), fmaxf(tmax[64 + c], tmax[96 + c])));
      const float Mn1 = fmaxf(Mr1, fmaxf(fmaxf(tmax[16 + c], tmax[48 + c]), fmaxf(tmax[80 + c], tmax[112 + c])));
      const float al0 = ex2((Mr0 - Mn0) * LOG2E), al1 = ex2((Mr1 - Mn1) * LOG2E);
      Mr0 = Mn0; Mr1 = Mn1;
      const float Mh = hh ? Mn1 : Mn0;
      {
        float p[8];
#pragma unroll
        for (int j = 0; j < 8; ++j) p[j] = ex2((sc[j] - Mh) * LOG2E);
        lsum = lsum * (hh ? al1 : al0) + (((p[0] + p[1]) + (p[2] + p[3])) + ((p[4] + p[5]) + (p[6] + p[7])));
#pragma unroll
        for (int t = 0; t < 2; ++t)
          *(u32x2*)(Pb + (16 * hh + c) * 256 + 16 * ((2 * (2 * kp + t) + (g >> 1)) ^ swz(c)) + 8 * (g & 1)) = u32x2{pack2(p[4 * t], p[4 * t + 1]), pack2(p[4 * t + 2], p[4 * t + 3])};
      }
#pragma unroll
      for (int ct = 0; ct < 4; ++ct) { acc[ct][0] *= al0; acc[ct][1] *= al1; }
      __syncthreads();
      {
        bf16x8 pa[2][4], pb[2][2];
#define PV_LOAD(B, KS) do { pb[B][0] = *(const bf16x8*)(Pb + c * 256 + 16 * ((4 * (KS) + g) ^ swz(c))); pb[B][1] = *(const bf16x8*)(Pb + (16 + c) * 256 + 16 * ((4 * (KS) + g) ^ swz(c))); \
          _Pragma("unroll") for (int ct = 0; ct < 4; ++ct) pa[B][ct] = tr_frag<1024>(smem, 4 * wave + ct, (KS), lane); } while (0)
#pragma unroll
        for (int ks = 0; ks < 4; ++ks) {
          PV_LOAD(0, ks);
          __builtin_amdgcn_sched_barrier(0);
#pragma unroll
          for (int ct = 0; ct < 4; ++ct) { acc[ct][0] = mfma16(pa[0][ct], pb[0][0], acc[ct][0]); acc[ct][1] = mfma16(pa[0][ct], pb[0][1], acc[ct][1]); }
          __builtin_amdgcn_sched_barrier(0);
        }
      }
    }
    STAMP(5);
    asm volatile("s_waitcnt vmcnt(0)" : "+v"(stg[0]), "+v"(stg[1]), "+v"(stg[2]), "+v"(stg[3]), "+v"(stg[4]), "+v"(stg[5]), "+v"(stg[6]), "+v"(stg[7]),
                 "+v"(stg[8]), "+v"(stg[9]), "+v"(stg[10]), "+v"(stg[11]), "+v"(stg[12]), "+v"(stg[13]), "+v"(stg[14]), "+v"(stg[15]) :: "memory");
    __syncthreads();
#pragma unroll
    for (int i = 0; i < 16; ++i) { const int row = 16 * wave + i; *(u32x4*)(smem + row * 1024 + 16 * (lane ^ swz(row))) = stg[i]; }
    __syncthreads();
    {
      f32x4 s0 = {0.f, 0.f, 0.f, 0.f}, s1 = {0.f, 0.f, 0.f, 0.f};
      {
        const int f = swz(c);
        const char* rb = smem + (32 * kp + c) * 1024;
        bf16x8 fa[2][4];
#define S_LOAD(B, H) do { _Pragma("unroll") for (int j = 0; j < 2; ++j) { const int s_ = 2 * (H) + j; \
          fa[B][2 * j] = *(const bf16x8*)(rb + 256 * (s_ >> 2) + 16 * ((4 * (s_ & 3) + g) ^ f)); fa[B][2 * j + 1] = *(const bf16x8*)(rb + 16384 + 256 * (s_ >> 2) + 16 * ((4 * (s_ & 3) + g) ^ f)); } } while (0)
        S_LOAD(0, 0);
#pragma unroll
        for (int hb = 0; hb < 8; ++hb) {
          if (hb < 7) S_LOAD((hb + 1) & 1, hb + 1);
          __builtin_amdgcn_sched_barrier(0);
#pragma unroll
          for (int j = 0; j < 2; ++j) { s0 = mfma16(fa[hb & 1][2 * j], qf[2 * hb + j], s0); s1 = mfma16(fa[hb & 1][2 * j + 1], qf[2 * hb + j], s1); }
          __builtin_amdgcn_sched_barrier(0);
        }
      }
      float sc[8];
#pragma unroll
      for (int j = 0; j < 4; ++j) { sc[j] = score_round(s0[j]); sc[4 + j] = score_round(s1[j]); }
      float tm = fmaxf(fmaxf(fmaxf(sc[0], sc[1]), fmaxf(sc[2], sc[3])), fmaxf(fmaxf(sc[4], sc[5]), fmaxf(sc[6], sc[7])));
      tm = xrow_max(tm);
      if (g == 0) tmax[kp * 32 + 16 * hh + c] = tm;
      __syncthreads();
      const float Mn0 = fmaxf(Mr0, fmaxf(fmaxf(tmax[c], tmax[32 + c]), fmaxf(tmax[64 + c], tmax[96 + c])));
      const float Mn1 = fmaxf(Mr1, fmaxf(fmaxf(tmax[16 + c], tmax[48 + c]), fmaxf(tmax[80 + c], tmax[112 + c])));
      const float al0 = ex2((Mr0 - Mn0) * LOG2E), al1 = ex2((Mr1 - Mn1) * LOG2E);
      Mr0 = Mn0; Mr1 = Mn1;
      const float Mh = hh ? Mn1 : Mn0;
      {
        float p[8];
#pragma unroll
        for (int j = 0; j < 8; ++j) p[j] = ex2((sc[j] - Mh) * LOG2E);
        lsum = lsum * (hh ? al1 : al0) + (((p[0] + p[1]) + (p[2] + p[3])) + ((p[4] + p[5]) + (p[6] + p[7])));
#pragma unroll
        for (int t = 0; t < 2; ++t)
          *(u32x2*)(Pb + (16 * hh + c) * 256 + 16 * ((2 * (2 * kp + t) + (g >> 1)) ^ swz(c)) + 8 * (g & 1)) = u32x2{pack2(p[4 * t], p[4 * t + 1]), pack2(p[4 * t + 2], p[4 * t + 3])};
      }
#pragma unroll
      for (int ct = 0; ct < 4; ++ct) { acc[ct][0] *= al0; acc[ct][1] *= al1; }
      __syncthreads();
      {
        bf16x8 pa[2][4], pb[2][2];
#define PV_LOAD(B, KS) do { pb[B][0] = *(const bf16x8*)(Pb + c * 256 + 16 * ((4 * (KS) + g) ^ swz(c))); pb[B][1] = *(const bf16x8*)(Pb + (16 + c) * 256 + 16 * ((4 * (KS) + g) ^ swz(c))); \
          _Pragma("unroll") for (int ct = 0; ct < 4; ++ct) pa[B][ct] = tr_frag<1024>(smem, 4 * wave + ct, (KS), lane); } while (0)
        PV_LOAD(0, 0);
#pragma unroll
        for (int ks = 0; ks < 4; ++ks) {
          if (ks < 3) PV_LOAD((ks + 1) & 1, ks + 1);
          __builtin_amdgcn_sched_barrier(0);
#pragma unroll
          for (int ct = 0; ct < 4; ++ct) { acc[ct][0] = mfma16(pa[ks & 1][ct], pb[ks & 1][0], acc[ct][0]); acc[ct][1] = mfma16(pa[ks & 1][ct], pb[ks & 1][1], acc[ct][1]); }
          __builtin_amdgcn_sched_barrier(0);
        }
      }
    }
    STAMP(3);
    { const float ls = xrow_sum(lsum); if (g == 0) tsum[kp * 32 + 16 * hh + c] = ls; }
#pragma unroll
    for (int ht = 0; ht < 2; ++ht) {
      unsigned cs = 0;
#pragma unroll
      for (int cp = 0; cp < 2; ++cp) {
        const u32x4 v = {pack2(acc[2 * cp][ht][0], acc[2 * cp][ht][1]) ^ key, pack2(acc[2 * cp][ht][2], acc[2 * cp][ht][3]) ^ key,
                         pack2(acc[2 * cp + 1][ht][0], acc[2 * cp + 1][ht][1]) ^ key, pack2(acc[2 * cp + 1][ht][2], acc[2 * cp + 1][ht][3]) ^ key};
        __builtin_amdgcn_raw_buffer_store_b128(v, wsr, (unsigned)(WS_APART + (((size_t)b * 32 + 16 * ht + c) * 32 + ck) * 1024 + 128 * wave + 64 * cp + 16 * g), 0, 16);
        cs += (v[0] + v[1]) + (v[2] + v[3]);
      }
      cs = x32_sumu(x16_sumu(cs));
      if (g == 0) __hip_atomic_fetch_add((LAS unsigned*)(csum + (wave >> 2) * 32 + 16 * ht + c), cs, __ATOMIC_RELAXED, __HIP_MEMORY_SCOPE_WORKGROUP);
    }
    RAWBAR();
    if (wave < 2) {
      const int head = 16 * hh + c;
      const float L = (tsum[head] + tsum[32 + head]) + (tsum[64 + head] + tsum[96 + head]);
      const u64 gv = g == 0 ? mk_gran(tagsh, hh ? Mr1 : Mr0) : (g == 1 ? mk_gran(tagsh, L) : (tagsh | (u64)cs_fold(csum[(g - 2) * 32 + head])));
      st_g((u64*)(ws + WS_ASTAT) + (((size_t)head * 2 + b) * 32 + ck) * 4 + g, gv);
    }
    STAMP(4);
    STAMP(6);
  }
}

extern "C" void kernel_launch(void* const* d_in, const int* in_sizes, int n_in, void* d_out, int out_size, void* d_ws, size_t ws_size, hipStream_t stream) {
  (void)hipFuncSetAttribute((const void*)fused_kernel, hipFuncAttributeMaxDynamicSharedMemorySize, LDS_BYTES);
  fused_kernel<<<256, NTHR, LDS_BYTES, stream>>>((const u16*)d_in[0], (const u16*)d_in[1], (const u16*)d_in[2], (const u16*)d_in[3], (const u16*)d_in[4],
      (const u16*)d_in[5], (const u16*)d_in[6], (const u16*)d_in[7], (const u16*)d_in[8], (const u16*)d_in[9], (const u16*)d_in[10], (u16*)d_out, (char*)d_ws);
}
```

```cpp
#include <hip/hip_runtime.h>
#include <stdint.h>

typedef unsigned short u16;
typedef unsigned long long u64;
typedef unsigned u32x4 __attribute__((ext_vector_type(4)));
typedef unsigned u32x2 __attribute__((ext_vector_type(2)));
typedef float f32x4 __attribute__((ext_vector_type(4)));
typedef short s16x4 __attribute__((ext_vector_type(4)));
typedef __attribute__((ext_vector_type(2))) __bf16 bf16x2;
typedef __attribute__((ext_vector_type(8))) __bf16 bf16x8;
#define GAS __attribute__((address_space(1)))
#define LAS __attribute__((address_space(3)))
#define DI __device__ __forceinline__
#define RLX_AGENT __ATOMIC_RELAXED, __HIP_MEMORY_SCOPE_AGENT
#define PSLEEP 4

constexpr float SCALE = 0.08838834764831845f;
constexpr float LOG2E = 1.4426950408889634f;
constexpr unsigned SPIN_CAP = 1u << 18;
constexpr int NW = 8, NTHR = 512;
constexpr int LDS_BYTES = 163840;

constexpr int NSLOT = 3;
constexpr size_t WS_EPOCH = 0;
constexpr size_t WS_QSEAL = 4096;
constexpr size_t WS_QABS  = 16384;
constexpr size_t WS_QAP   = WS_QABS + 65536;
constexpr size_t WS_QBP   = WS_QAP + 524288;
constexpr size_t WS_ASTAT = WS_QBP + 524288;
constexpr size_t WS_BPART = WS_ASTAT + 65536;
constexpr size_t WS_YA    = WS_BPART + 64 * NSLOT * 144 * 8;
constexpr size_t WS_YB    = WS_YA + 524288;
constexpr size_t WS_APART = WS_YB + 524288;
constexpr size_t WS_END   = WS_APART + 2097152;

DI float bf_lo(unsigned x) { return __uint_as_float(x << 16); }
DI float bf_hi(unsigned x) { return __uint_as_float(x & 0xffff0000u); }
DI unsigned f2bf(float f) { unsigned u = __float_as_uint(f); u += 0x7fffu + ((u >> 16) & 1u); return u >> 16; }
DI unsigned pack2(float lo, float hi) { typedef __attribute__((ext_vector_type(2))) float f2_; return __builtin_bit_cast(unsigned, __builtin_convertvector(f2_{lo, hi}, bf16x2)); }
DI float rbf(float f) { return __uint_as_float(pack2(f, f) << 16); }
DI float dot2(unsigned a, unsigned b, float acc) {
  return __builtin_amdgcn_fdot2_f32_bf16(__builtin_bit_cast(bf16x2, a), __builtin_bit_cast(bf16x2, b), acc, false);
}
template <int CTRL> DI float dpp(float x) {
  return __builtin_bit_cast(float, __builtin_amdgcn_mov_dpp(__builtin_bit_cast(int, x), CTRL, 0xf, 0xf, true));
}
constexpr int XOR1 = 0xB1, XOR2 = 0x4E, XOR7 = 0x141, XOR8 = 0x128;
DI float row16_sum(float x) { x += dpp<XOR8>(x); x += dpp<XOR7>(x); x += dpp<XOR2>(x); x += dpp<XOR1>(x); return x; }
DI float row16_max(float x) { x = fmaxf(x, dpp<XOR8>(x)); x = fmaxf(x, dpp<XOR7>(x)); x = fmaxf(x, dpp<XOR2>(x)); x = fmaxf(x, dpp<XOR1>(x)); return x; }
DI float xrow_max(float x) {
  auto s = __builtin_amdgcn_permlane16_swap(__float_as_uint(x), __float_as_uint(x), false, false);
  x = fmaxf(__uint_as_float(s[0]), __uint_as_float(s[1]));
  auto t = __builtin_amdgcn_permlane32_swap(__float_as_uint(x), __float_as_uint(x), false, false);
  return fmaxf(__uint_as_float(t[0]), __uint_as_float(t[1]));
}
DI float xrow_sum(float x) {
  auto s = __builtin_amdgcn_permlane16_swap(__float_as_uint(x), __float_as_uint(x), false, false);
  x = __uint_as_float(s[0]) + __uint_as_float(s[1]);
  auto t = __builtin_amdgcn_permlane32_swap(__float_as_uint(x), __float_as_uint(x), false, false);
  return __uint_as_float(t[0]) + __uint_as_float(t[1]);
}
DI float x32_sum(float x) {
  auto t = __builtin_amdgcn_permlane32_swap(__float_as_uint(x), __float_as_uint(x), false, false);
  return __uint_as_float(t[0]) + __uint_as_float(t[1]);
}
template <int CTRL> DI unsigned dppu(unsigned x) { return (unsigned)__builtin_amdgcn_mov_dpp((int)x, CTRL, 0xf, 0xf, true); }
DI unsigned row16_sumu(unsigned x) { x += dppu<XOR8>(x); x += dppu<XOR7>(x); x += dppu<XOR2>(x); x += dppu<XOR1>(x); return x; }
DI unsigned x16_sumu(unsigned x) { auto s = __builtin_amdgcn_permlane16_swap(x, x, false, false); return s[0] + s[1]; }
DI unsigned x32_sumu(unsigned x) { auto s = __builtin_amdgcn_permlane32_swap(x, x, false, false); return s[0] + s[1]; }
DI unsigned call_key(u64 tagsh) { unsigned k = (unsigned)(tagsh >> 24) * 0x9E3779B1u; k ^= k >> 15; return k * 0x85EBCA77u + 0x6A09E667u; }
DI unsigned cs_fold(unsigned s) { return (s ^ (s >> 24) * 0x9E37u) & 0xFFFFFFu; }
DI u32x4 ldnt(const void* p) { return __builtin_nontemporal_load((const u32x4*)p); }
DI u64 ld_g(const void* p) { return __hip_atomic_load((GAS u64*)p, RLX_AGENT); }
DI void st_g(void* p, u64 v) { __hip_atomic_store((GAS u64*)p, v, RLX_AGENT); }
DI u64 mk_gran(u64 tagsh, float v) { unsigned u = __float_as_uint(v); u += 0x7fu + ((u >> 8) & 1u); return tagsh | (u64)(u >> 8); }
DI bool gran_ok(u64 g, u64 tagsh) { return (g & 0xFFFFFFFFFF000000ull) == tagsh; }
DI float gran_val(u64 g) { return __uint_as_float(((unsigned)g & 0xFFFFFFu) << 8); }
DI int swz(int row) { return ((row & 3) << 1) | (row & 8); }
DI float score_round(float d) { return rbf(rbf(d) * SCALE); }
DI float ex2(float x) { return __builtin_amdgcn_exp2f(x); }
DI void st8(float* p, const float (&a)[8]) { *(f32x4*)p = f32x4{a[0], a[1], a[2], a[3]}; *(f32x4*)(p + 4) = f32x4{a[4], a[5], a[6], a[7]}; }
DI void dma16(const void* g, char* l) {
  unsigned keep; const unsigned dst = __builtin_amdgcn_readfirstlane((unsigned)(size_t)(LAS char*)l);
  asm volatile("s_mov_b32 %0, m0\n\ts_mov_b32 m0, %2\n\ts_nop 0\n\tglobal_load_lds_dwordx4 %1, off nt\n\ts_mov_b32 m0, %0" : "=&s"(keep) : "v"(g), "s"(dst) : "memory");
}
#define VMWAIT0() asm volatile("s_waitcnt vmcnt(0)" ::: "memory")
DI f32x4 mfma16(bf16x8 a, bf16x8 b, f32x4 c) { return __builtin_amdgcn_mfma_f32_16x16x32_bf16(a, b, c, 0, 0, 0); }

template <int ROWB> DI bf16x8 tr_frag(const char* img, int nt, int ks, int lane) {
  const int g = lane >> 4, q4 = (lane & 15) >> 2, p4 = lane & 3;
  const int row = 32 * ks + 8 * g + q4, ch = 2 * nt + (p4 >> 1);
  const char* p = img + row * ROWB + 16 * (ch ^ swz(row)) + 8 * (p4 & 1);
  const s16x4 lo = __builtin_amdgcn_ds_read_tr16_b64_v4i16((LAS s16x4*)p);
  const s16x4 hi = __builtin_amdgcn_ds_read_tr16_b64_v4i16((LAS s16x4*)(p + 4 * ROWB));
  return __builtin_bit_cast(bf16x8, __builtin_shufflevector(lo, hi, 0, 1, 2, 3, 4, 5, 6, 7));
}
template <int NC> DI bf16x8 x_frag(const char* xf, int stride, int ks, int lane) {
  const int g = lane >> 4, c = lane & 15;
  u32x4 v = {0u, 0u, 0u, 0u};
  if (c < NC) v = *(const u32x4*)(xf + c * stride + 64 * ks + 16 * g);
  return __builtin_bit_cast(bf16x8, v);
}
#define STR2(x) #x
#define STR(x) STR2(x)
#define DTHR 5
#define DTHROTTLE() asm volatile("s_waitcnt vmcnt(" STR(DTHR) ")" ::: "memory")
template <bool TH = true> DI void dma_rows256(const char* gbase, size_t gstride, char* img, int row0, int n, int lane) {
  const int rr = lane >> 4, pos = lane & 15;
#pragma unroll 4
  for (int i = 0; i < n; ++i) { const int row = row0 + 4 * i + rr; if (TH) DTHROTTLE(); dma16(gbase + (size_t)row * gstride + 16 * (pos ^ swz(row)), img + (row0 + 4 * i) * 256); }
}
template <bool TH = true> DI void dma_rows1k(const char* gbase, char* img, int row0, int n, int lane) {
#pragma unroll 4
  for (int i = 0; i < n; ++i) { const int row = row0 + i; if (TH) DTHROTTLE(); dma16(gbase + (size_t)row * 1024 + 16 * (lane ^ swz(row)), img + row * 1024); }
}

struct BState { float m, l; float acc[8]; };
DI unsigned cvtpk(float lo, float hi) { typedef __attribute__((ext_vector_type(2))) float f2; typedef __attribute__((ext_vector_type(2))) __bf16 b2;
  return __builtin_bit_cast(unsigned, __builtin_convertvector(f2{lo, hi}, b2)); }
DI void b_step2(const u32x4& k0, const u32x4& v0, const u32x4& k1, const u32x4& v1, const u32x4& q, BState& st) {
  float d0 = dot2(k0[0], q[0], 0.f), d1 = dot2(k1[0], q[0], 0.f);
#pragma unroll
  for (int j = 1; j < 4; ++j) { d0 = dot2(k0[j], q[j], d0); d1 = dot2(k1[j], q[j], d1); }
  d0 = row16_sum(d0); d1 = row16_sum(d1);
  unsigned r = cvtpk(d0, d1);
  r = cvtpk(bf_lo(r) * SCALE, bf_hi(r) * SCALE);
  const float s0 = bf_lo(r), s1 = bf_hi(r), sm = fmaxf(s0, s1);
  if (__any(sm > st.m)) {
    const float mn = fmaxf(st.m, sm);
    const float alpha = ex2((st.m - mn) * LOG2E);
    st.m = mn; st.l *= alpha;
#pragma unroll
    for (int j = 0; j < 8; ++j) st.acc[j] *= alpha;
  }
  const float ml = st.m * LOG2E;
  const float p0 = ex2(fmaf(s0, LOG2E, -ml)), p1 = ex2(fmaf(s1, LOG2E, -ml));
  st.l += p0 + p1;
#pragma unroll
  for (int j = 0; j < 4; ++j) {
    st.acc[2 * j] = fmaf(p1, bf_lo(v1[j]), fmaf(p0, bf_lo(v0[j]), st.acc[2 * j]));
    st.acc[2 * j + 1] = fmaf(p1, bf_hi(v1[j]), fmaf(p0, bf_hi(v0[j]), st.acc[2 * j + 1]));
  }
}
#define RAWBAR() do { asm volatile("s_waitcnt lgkmcnt(0)" ::: "memory"); __builtin_amdgcn_s_barrier(); asm volatile("" ::: "memory"); } while (0)
#define RD 4
#define PU0 7
#define PU1 8
#define LDKV(K_, V_, OFF) asm volatile("global_load_dwordx4 %0, %2, %3 nt\n\tglobal_load_dwordx4 %1, %2, %4 nt" : "=&v"(K_), "=&v"(V_) : "v"(OFF), "s"(Kc), "s"(Vc) : "memory")
#define WAITKV2(K0, V0, K1, V1, N) asm volatile("s_waitcnt vmcnt(%4)" : "+v"(K0), "+v"(V0), "+v"(K1), "+v"(V1) : "i"(N) : "memory")
DI void kv_stream(const u16* Kc, const u16* Vc, char* ws, const __amdgpu_buffer_rsrc_t& wsr, char* lds  , int pair, int u0, int nu, int slot, u64 tagsh, int tid) {
  const int lane = tid & 63, wave = __builtin_amdgcn_readfirstlane(tid >> 6), g = lane >> 4, c = lane & 15;
  const unsigned base = (unsigned)pair * 2097152u + (unsigned)((6144 + 128 * u0 + 16 * wave + g) * 256 + 16 * c);
#define STEP_OFF(S) ((unsigned)(((S) >> 2) * 32768 + ((S) & 3) * 1024))
  unsigned* qs = (unsigned*)(lds + 8 * 528);
  u32x4 kr[RD], vr[RD];
#pragma unroll
  for (int i = 0; i < RD; ++i) LDKV(kr[i], vr[i], base + STEP_OFF(i));
  if (wave == 0) {
    const unsigned qo = (unsigned)WS_QBP + (unsigned)((pair & 31) * 2048 + (pair >> 5) * 128 + 2 * lane) * 8u;
    const unsigned th = (unsigned)(tagsh >> 32), tl = (unsigned)tagsh;
    float s0, s1;
#pragma unroll 1
    for (unsigned spins = 0; spins < SPIN_CAP; ++spins) {
      asm volatile("" ::: "memory");
      u32x4 v[8]; bool ok = true; s0 = 0.f; s1 = 0.f;
#pragma unroll
      for (int k = 0; k < 8; ++k) v[k] = __builtin_amdgcn_raw_buffer_load_b128(wsr, qo + 2048u * k, 0, 16);
#pragma unroll
      for (int k = 0; k < 8; ++k) {
        ok = ok && v[k][1] == th && (v[k][0] & 0xFF000000u) == tl && v[k][3] == th && (v[k][2] & 0xFF000000u) == tl;
        s0 += __uint_as_float(v[k][0] << 8); s1 += __uint_as_float(v[k][2] << 8);
      }
      if (__all(ok)) break;
      __builtin_amdgcn_s_sleep(2);
    }
    qs[lane] = pack2(s0, s1);
  }
  RAWBAR();
  const u32x4 q = *(const u32x4*)(qs + 4 * c);
  BState st; st.m = -INFINITY; st.l = 0.f;
#pragma unroll
  for (int j = 0; j < 8; ++j) st.acc[j] = 0.f;
  const int ns = 4 * nu;
  int s = 0;
#pragma unroll 1
  for (; s + RD < ns; s += RD) {
#pragma unroll
    for (int i = 0; i < RD; i += 2) {
      WAITKV2(kr[i], vr[i], kr[i + 1], vr[i + 1], 2 * (RD - 2));
      b_step2(kr[i], vr[i], kr[i + 1], vr[i + 1], q, st);
      __builtin_amdgcn_sched_barrier(0);
      const unsigned o = base + STEP_OFF(s + i + RD);
      LDKV(kr[i], vr[i], o);
      LDKV(kr[i + 1], vr[i + 1], o + 1024u);
    }
  }
#pragma unroll
  for (int i = 0; i < RD; i += 2) {
    WAITKV2(kr[i], vr[i], kr[i + 1], vr[i + 1], 2 * (RD - 2 - i));
    b_step2(kr[i], vr[i], kr[i + 1], vr[i + 1], q, st);
  }
  {
    const float Mw = xrow_max(st.m);
    const float f = ex2((st.m - Mw) * LOG2E);
#pragma unroll
    for (int j = 0; j < 8; ++j) st.acc[j] = xrow_sum(st.acc[j] * f);
    const float l = xrow_sum(st.l * f);
    float* rec = (float*)lds + wave * 132;
    if (g == 0) { st8(rec + 4 + 8 * c, st.acc); if (c == 0) { rec[0] = Mw; rec[1] = l; } }
  }
  RAWBAR();
  if (tid < 128) {
    const float* recs = (const float*)lds;
    float M = recs[0];
#pragma unroll
    for (int w = 1; w < NW; ++w) M = fmaxf(M, recs[w * 132]);
    float o = 0.f, L = 0.f;
#pragma unroll
    for (int w = 0; w < NW; ++w) { const float wt = ex2((recs[w * 132] - M) * LOG2E); o = fmaf(wt, recs[w * 132 + 4 + tid], o); L = fmaf(wt, recs[w * 132 + 1], L); }
    u64* sl = (u64*)(ws + WS_BPART) + ((size_t)pair * NSLOT + slot) * 144;
    st_g(sl + tid, mk_gran(tagsh, o));
    if (tid < 2) st_g(sl + 128 + tid, mk_gran(tagsh, tid ? L : M));
  }
}

DI void stage1_compute(const char* img, const char* xf, u64* dst, u64 tagsh, int tid) {
  const int lane = tid & 63, wave = tid >> 6;
  const bf16x8 a0 = tr_frag<256>(img, wave, 0, lane), a1 = tr_frag<256>(img, wave, 1, lane), x0 = x_frag<2>(xf, 128, 0, lane), x1 = x_frag<2>(xf, 128, 1, lane);
  __builtin_amdgcn_sched_barrier(0);
  f32x4 d = mfma16(a0, x0, f32x4{0.f, 0.f, 0.f, 0.f});
  d = mfma16(a1, x1, d);
  const int b = (lane >> 4) & 1, n = lane & 15, srcl = 16 * (n >> 2) + b;
  float v = 0.f;
#pragma unroll
  for (int r = 0; r < 4; ++r) { const float t0 = __shfl(d[r], srcl); if ((n & 3) == r) v = t0; }
  if (lane < 32) st_g(dst + b * 128 + 16 * wave + n, mk_gran(tagsh, v));
}

DI void out_stage(const float* ybuf, float* red, size_t yoff, const __amdgpu_buffer_rsrc_t& wsr, u16* out, int srv, int colbase, u64 tagsh, int tid) {
  const int lane = tid & 63, wave = tid >> 6;
  {
    const u64 ga = mk_gran(tagsh, ybuf[2 * tid]), gb = mk_gran(tagsh, ybuf[2 * tid + 1]);
    __builtin_amdgcn_raw_buffer_store_b128(u32x4{(unsigned)ga, (unsigned)(ga >> 32), (unsigned)gb, (unsigned)(gb >> 32)}, wsr, (unsigned)(yoff + (size_t)srv * 8192 + 16 * tid), 0, 16);
  }
  const int b = srv >> 5, m0 = (srv & 31) * 16;
  const unsigned off = (unsigned)(yoff + (size_t)(tid >> 3) * 8192 + (b * 512 + m0 + 2 * (tid & 7)) * 8);
  const unsigned th = (unsigned)(tagsh >> 32), tl = (unsigned)tagsh;
  u32x4 v; unsigned spins = 0;
#pragma unroll 1
  for (;;) {
    asm volatile("" ::: "memory");
    v = __builtin_amdgcn_raw_buffer_load_b128(wsr, off, 0, 16);
    if (__all(v[1] == th && (v[0] & 0xFF000000u) == tl && v[3] == th && (v[2] & 0xFF000000u) == tl)) break;
    if (++spins > SPIN_CAP) break;
  }
  float f0 = __uint_as_float(v[0] << 8), f1 = __uint_as_float(v[2] << 8);
  f0 += dpp<XOR8>(f0); f1 += dpp<XOR8>(f1);
  f0 = xrow_sum(f0); f1 = xrow_sum(f1);
  if (lane < 8) { red[wave * 16 + 2 * lane] = f0; red[wave * 16 + 2 * lane + 1] = f1; }
  __syncthreads();
  if (tid < 16) {
    float s = 0.f;
#pragma unroll
    for (int w = 0; w < NW; ++w) s += red[w * 16 + tid];
    out[b * 1024 + colbase + m0 + tid] = (u16)f2bf(s);
  }
}

__global__ void __launch_bounds__(NTHR) fused_kernel(
    const u16* __restrict__ Ax, const u16* __restrict__ ckv, const u16* __restrict__ Awq, const u16* __restrict__ Awuk,
    const u16* __restrict__ Awuv, const u16* __restrict__ Awo, const u16* __restrict__ Bx, const u16* __restrict__ Kc,
    const u16* __restrict__ Vc, const u16* __restrict__ Bwq, const u16* __restrict__ Bwo, u16* __restrict__ out, char* __restrict__ ws) {
  extern __shared__ __attribute__((aligned(16))) char smem[];
  const int tid = threadIdx.x, lane = tid & 63, wave = __builtin_amdgcn_readfirstlane(tid >> 6);
  const int g = lane >> 4, c = lane & 15;
  const int bid = blockIdx.x, xg = bid & 7, idx = bid >> 3;
  asm volatile("" :: "s"(Ax), "s"(ckv), "s"(Awq), "s"(Awuk), "s"(Awuv), "s"(Awo), "s"(Bx), "s"(Kc), "s"(Vc), "s"(Bwq), "s"(Bwo), "s"(out), "s"(ws));
#define GET_TAG() u64 epoch; asm volatile("s_load_dwordx2 %0, %1, 0x0\n\ts_waitcnt lgkmcnt(0)" : "=&s"(epoch) : "s"(ws) : "memory"); const u64 tagsh = (epoch + 1) << 24
  const __amdgpu_buffer_rsrc_t wsr = __builtin_amdgcn_make_buffer_rsrc(ws, 0, (int)WS_END, 0x00020000);
  char* aux = smem + 131072;
  char* s1img = aux;
  char* s1x = smem + 131072 - 512;
  const int h1 = 4 * xg + (idx >> 3), j1 = idx & 7;
  if (tid < 32) dma16((tid < 16 ? Ax : Bx) + ((tid >> 3) & 1) * 512 + 64 * j1 + 8 * (tid & 7), s1x);
  dma_rows256<false>((const char*)(Awq + ((size_t)64 * j1 * 32 + h1) * 128), 8192, s1img, 8 * wave, 2, lane);
  dma_rows256<false>((const char*)(Bwq + (size_t)64 * j1 * 4096 + h1 * 128), 8192, s1img + 16384, 8 * wave, 2, lane);
#define S1_WAIT(N) do { asm volatile("s_waitcnt vmcnt(%0) lgkmcnt(0)" :: "i"(N) : "memory"); __builtin_amdgcn_s_barrier(); asm volatile("" ::: "memory"); } while (0)
#define STAGE1(NL) do { S1_WAIT((NL) + 2); stage1_compute(s1img, s1x, (u64*)(ws + WS_QAP) + ((size_t)h1 * 8 + j1) * 256, tagsh, tid); \
    S1_WAIT((NL) + 1); stage1_compute(s1img + 16384, s1x + 256, (u64*)(ws + WS_QBP) + ((size_t)h1 * 8 + j1) * 256, tagsh, tid); RAWBAR(); } while (0)

  if (idx < 8) {
    const int h = 4 * xg + (idx >> 1), half = idx & 1, srv = h * 2 + half;
    char* xf = aux;
    char* qf = aux + 8448;
    char* xf2 = aux + 18432;
    float* rec = (float*)(aux + 9216);
    float* ybuf = (float*)(aux + 13824);
    float* red = (float*)(aux + 17920);
    dma_rows256<false>((const char*)(Awuk + ((size_t)256 * half * 32 + h) * 128), 8192, smem, 32 * wave, 8, lane);
    GET_TAG();
    const unsigned key = call_key(tagsh);
    STAGE1(8);
    VMWAIT0();
    bf16x8 wkf[2][4];
#pragma unroll
    for (int t = 0; t < 2; ++t)
#pragma unroll
      for (int s = 0; s < 4; ++s) wkf[t][s] = *(const bf16x8*)(smem + (32 * wave + 16 * t + c) * 256 + 16 * ((4 * s + g) ^ swz(c)));
#define SUM8(QP, S) do { u64 gq[8]; unsigned spins = 0; \
      for (;;) { bool ok = true; _Pragma("unroll") for (int k = 0; k < 8; ++k) gq[k] = ld_g((QP) + k * 256); \
        _Pragma("unroll") for (int k = 0; k < 8; ++k) ok = ok && gran_ok(gq[k], tagsh); \
        if (__all(ok)) break; if (++spins > SPIN_CAP) break; __builtin_amdgcn_s_sleep(1); } \
      S = 0.f; _Pragma("unroll") for (int k = 0; k < 8; ++k) S += gran_val(gq[k]); } while (0)
    if (wave < 4) {
      float s; SUM8((const u64*)(ws + WS_QAP) + (size_t)h * 2048 + tid, s);
      *(u16*)(qf + (tid >> 7) * 272 + 2 * (tid & 127)) = (u16)f2bf(s);
    }
    __syncthreads();
    {
      bf16x8 qb[4];
#pragma unroll
      for (int s = 0; s < 4; ++s) qb[s] = x_frag<2>(qf, 272, s, lane);
      __builtin_amdgcn_sched_barrier(0);
      unsigned cs = 0;
#pragma unroll
      for (int t = 0; t < 2; ++t) {
        f32x4 d = {0.f, 0.f, 0.f, 0.f};
#pragma unroll
        for (int s = 0; s < 4; ++s) d = mfma16(wkf[t][s], qb[s], d);
        const unsigned w0 = pack2(d[0], d[1]) ^ key, w1 = pack2(d[2], d[3]) ^ key;
        cs += w0 + w1;
        if (c < 2) {
          const size_t off = WS_QABS + ((((size_t)c * 2 + (h >> 4)) * 16 + 8 * half + wave) * 64 + (2 * t + (g >> 1)) * 16 + (h & 15)) * 16 + 8 * (g & 1);
          st_g(ws + off, (u64)w0 | ((u64)w1 << 32));
        }
      }
      cs = x32_sumu(x16_sumu(cs));
      if (lane < 2) st_g((u64*)(ws + WS_QSEAL) + (((size_t)lane * 2 + (h >> 4)) * 16 + 8 * half + wave) * 16 + (h & 15), tagsh | (u64)cs_fold(cs));
    }
    VMWAIT0();
    RAWBAR();
    dma_rows1k<false>((const char*)(Awo + (size_t)h * 128 * 512), smem, 16 * wave, 16, lane);
    S1_WAIT(0);
    bf16x8 wof[4][4];
#pragma unroll
    for (int t = 0; t < 4; ++t)
#pragma unroll
      for (int k = 0; k < 4; ++k) wof[t][k] = tr_frag<1024>(smem, 4 * wave + t, k, lane);
    RAWBAR();
    dma_rows256<false>((const char*)(Awuv + ((size_t)256 * half * 32 + h) * 128), 8192, smem, 32 * wave, 8, lane);
    kv_stream(Kc, Vc, ws, wsr, aux + 24576, half * 32 + h, PU0 + PU1, 16 - PU0 - PU1, 2, tagsh, tid);
    {
      const int b = wave >> 2, ck0 = 8 * (wave & 3);
      const u64* sp = (const u64*)(ws + WS_ASTAT) + (((size_t)h * 2 + b) * 32 + ck0) * 4 + (lane & 31);
      u64 gs; unsigned spins = 0;
#pragma unroll 1
      for (;;) { gs = ld_g(sp); if (__any(gran_ok(gs, tagsh))) break; if (++spins > SPIN_CAP) break; __builtin_amdgcn_s_sleep(PSLEEP); }
      const int hi = lane >> 5, l5 = lane & 31;
      u32x4 pr[4];
#pragma unroll 1
      for (;;) {
        asm volatile("" ::: "memory");
        gs = ld_g(sp);
#pragma unroll
        for (int i = 0; i < 4; ++i)
          pr[i] = __builtin_amdgcn_raw_buffer_load_b128(wsr, (unsigned)(WS_APART + ((((size_t)b * 32 + h) * 32 + ck0 + 2 * i + hi) * 512 + 256 * half + 8 * l5) * 2), 0, 16);
        bool ok = gran_ok(gs, tagsh);
#pragma unroll
        for (int i = 0; i < 4; ++i) {
          const unsigned cs = cs_fold(x16_sumu(row16_sumu((pr[i][0] + pr[i][1]) + (pr[i][2] + pr[i][3]))));
          const unsigned elo = (unsigned)__builtin_amdgcn_readlane((int)(unsigned)gs, 8 * i + 2 + half) & 0xFFFFFFu, ehi = (unsigned)__builtin_amdgcn_readlane((int)(unsigned)gs, 8 * i + 6 + half) & 0xFFFFFFu;
          ok = ok && cs == (hi ? ehi : elo);
        }
        if (__all(ok)) break;
        if (++spins > SPIN_CAP) break;
      }
      const float sv = gran_val(gs);
      const float Mw = xrow_max(row16_max((lane & 3) ? -INFINITY : sv));
      const float we = ex2((sv - Mw) * LOG2E);
      const float wl = dpp<XOR1>(we) * sv;
      const float Lw = xrow_sum(row16_sum(((lane & 3) == 1 && lane < 32) ? wl : 0.f));
      float a[8];
#pragma unroll
      for (int j = 0; j < 8; ++j) a[j] = 0.f;
#pragma unroll
      for (int i = 0; i < 4; ++i) {
        const float wlo = __builtin_bit_cast(float, __builtin_amdgcn_readlane(__builtin_bit_cast(int, we), 8 * i));
        const float whi = __builtin_bit_cast(float, __builtin_amdgcn_readlane(__builtin_bit_cast(int, we), 8 * i + 4));
        const float wt = hi ? whi : wlo;
#pragma unroll
        for (int j = 0; j < 4; ++j) { const unsigned x = pr[i][j] ^ key; a[2 * j] = fmaf(wt, bf_lo(x), a[2 * j]); a[2 * j + 1] = fmaf(wt, bf_hi(x), a[2 * j + 1]); }
      }
#pragma unroll
      for (int j = 0; j < 8; ++j) a[j] = x32_sum(a[j]);
      if (hi == 0) {
        const int c0 = ((8 * l5) & 0xE0) | (((8 * l5) & 0x18) >> 1);
        float hv[8];
#pragma unroll
        for (int j = 0; j < 8; ++j) hv[j] = rbf(a[j]);
        char* xh = xf + (2 * wave) * 528 + 2 * c0; char* xl = xh + 528;
        *(u32x2*)xh = u32x2{pack2(hv[0], hv[1]), pack2(hv[2], hv[3])}; *(u32x2*)(xh + 32) = u32x2{pack2(hv[4], hv[5]), pack2(hv[6], hv[7])};
        *(u32x2*)xl = u32x2{pack2(a[0] - hv[0], a[1] - hv[1]), pack2(a[2] - hv[2], a[3] - hv[3])}; *(u32x2*)(xl + 32) = u32x2{pack2(a[4] - hv[4], a[5] - hv[5]), pack2(a[6] - hv[6], a[7] - hv[7])};
      }
      if (lane == 0) { rec[2 * wave] = Mw; rec[2 * wave + 1] = Lw; }
    }
    VMWAIT0();
    __syncthreads();
    {
      bf16x8 wvf[8], xb8[8];
#pragma unroll
      for (int ks = 0; ks < 8; ++ks) { wvf[ks] = tr_frag<256>(smem, wave, ks, lane); xb8[ks] = *(const bf16x8*)(xf + c * 528 + 64 * ks + 16 * g); }
      const float* rb4 = rec + 8 * (c >> 3);
      const float M = fmaxf(fmaxf(rb4[0], rb4[2]), fmaxf(rb4[4], rb4[6]));
      float L = 0.f;
#pragma unroll
      for (int w = 0; w < 4; ++w) L = fmaf(ex2((rb4[2 * w] - M) * LOG2E), rb4[2 * w + 1], L);
      const float wt = ex2((rec[2 * (c >> 1)] - M) * LOG2E) / L;
      __builtin_amdgcn_sched_barrier(0);
      f32x4 d = {0.f, 0.f, 0.f, 0.f};
#pragma unroll
      for (int ks = 0; ks < 8; ++ks) d = mfma16(wvf[ks], xb8[ks], d);
#pragma unroll
      for (int j = 0; j < 4; ++j) { float v = d[j] * wt; v += dpp<XOR1>(v); v += dpp<XOR2>(v); v += dpp<XOR7>(v); d[j] = v; }
      if ((c & 7) == 0) {
        const int bb = c >> 3, dk = (16 * wave + 4 * g) * 2;
        const float h0 = rbf(d[0]), h1_ = rbf(d[1]), h2 = rbf(d[2]), h3 = rbf(d[3]);
        *(u32x2*)(xf2 + bb * 272 + dk) = u32x2{pack2(h0, h1_), pack2(h2, h3)};
        *(u32x2*)(xf2 + (2 + bb) * 272 + dk) = u32x2{pack2(d[0] - h0, d[1] - h1_), pack2(d[2] - h2, d[3] - h3)};
      }
    }
    __syncthreads();
    {
      bf16x8 xb[4];
#pragma unroll
      for (int k = 0; k < 4; ++k) xb[k] = x_frag<4>(xf2, 272, k, lane);
#pragma unroll
      for (int t = 0; t < 4; ++t) {
        f32x4 d = {0.f, 0.f, 0.f, 0.f};
#pragma unroll
        for (int k = 0; k < 4; ++k) d = mfma16(wof[t][k], xb[k], d);
#pragma unroll
        for (int j = 0; j < 4; ++j) d[j] += dpp<XOR2>(d[j]);
        if (c < 2) *(f32x4*)(ybuf + c * 512 + 16 * (4 * wave + t) + 4 * g) = d;
      }
    }
    __syncthreads();
    out_stage(ybuf, red, WS_YA, wsr, out, srv, 0, tagsh, tid);
    if (srv == 0 && tid == 0) st_g(ws + WS_EPOCH, epoch + 1);
  } else if (idx < 24) {
    const int p16 = idx - 8, pair = ((p16 >> 1) & 1) * 32 + 4 * xg + (p16 >> 2), part = p16 & 1;
    const int h = 4 * xg + (p16 >> 2), half = (p16 >> 1) & 1, srv = h * 2 + half;
    GET_TAG();
    STAGE1(0);
    if (part == 0) dma_rows1k<false>((const char*)(Bwo + ((size_t)h * 128 + 64 * half) * 512), smem + 65536, 8 * wave, 8, lane);
    kv_stream(Kc, Vc, ws, wsr, smem, pair, part ? PU0 : 0, part ? PU1 : PU0, part, tagsh, tid);
    if (part == 0) {
    char* xf2 = aux + 18432;
    float* ybuf = (float*)(aux + 13824);
    float* red = (float*)(aux + 17920);
    bf16x8 wof[4][2];
#pragma unroll
    for (int t = 0; t < 4; ++t) { wof[t][0] = tr_frag<1024>(smem + 65536, 4 * wave + t, 0, lane); wof[t][1] = tr_frag<1024>(smem + 65536, 4 * wave + t, 1, lane); }
    if (wave < 2) {
      const int b = wave;
      const u64* base = (const u64*)(ws + WS_BPART) + (size_t)(b * 32 + h) * NSLOT * 144;
      u64 go[NSLOT], gs; unsigned spins = 0;
      const int sl = min(lane >> 1, NSLOT - 1);
#pragma unroll 1
      for (;;) {
        gs = ld_g(base + sl * 144 + 128 + (lane & 1));
#pragma unroll
        for (int s = 0; s < NSLOT; ++s) go[s] = ld_g(base + s * 144 + 64 * half + lane);
        bool ok = gran_ok(gs, tagsh);
#pragma unroll
        for (int s = 0; s < NSLOT; ++s) ok = ok && gran_ok(go[s], tagsh);
        if (__all(ok)) break;
        if (++spins > SPIN_CAP) break;
        __builtin_amdgcn_s_sleep(1);
      }
      const float sv = gran_val(gs);
      const float M = xrow_max(row16_max((lane & 1) ? -INFINITY : sv));
      const float we = ex2((sv - M) * LOG2E);
      const float wl = dpp<XOR1>(we) * sv;
      const float L = xrow_sum(row16_sum(((lane & 1) && lane < 2 * NSLOT) ? wl : 0.f));
      float o = 0.f;
#pragma unroll
      for (int s = 0; s < NSLOT; ++s) o = fmaf(__builtin_bit_cast(float, __builtin_amdgcn_readlane(__builtin_bit_cast(int, we), 2 * s)), gran_val(go[s]), o);
      o /= L;
      *(u16*)(xf2 + b * 272 + 2 * lane) = (u16)f2bf(o);
    }
    __syncthreads();
    {
      const bf16x8 b0 = x_frag<2>(xf2, 272, 0, lane), b1 = x_frag<2>(xf2, 272, 1, lane);
#pragma unroll
      for (int t = 0; t < 4; ++t) {
        f32x4 d = mfma16(wof[t][0], b0, f32x4{0.f, 0.f, 0.f, 0.f}); d = mfma16(wof[t][1], b1, d);
        if (c < 2) *(f32x4*)(ybuf + c * 512 + 16 * (4 * wave + t) + 4 * g) = d;
      }
    }
    __syncthreads();
    out_stage(ybuf, red, WS_YB, wsr, out, srv, 512, tagsh, tid);
    }
  } else {
    const int a8 = idx - 24, a = xg * 8 + a8, b = a >> 5, ck = a & 31;
    const char* crow = (const char*)(ckv + ((size_t)b * 8192 + ck * 256) * 512);
    dma_rows1k<false>(crow, smem, 16 * wave, 2, lane);
    GET_TAG();
    STAGE1(2);
    dma_rows1k(crow, smem, 16 * wave + 2, 14, lane);
    const unsigned key = call_key(tagsh);
    {
      const u64* sp = (const u64*)(ws + WS_QSEAL) + (size_t)b * 512 + 64 * wave + lane;
      unsigned spins = 0;
      u32x4 t[4];
#pragma unroll 1
      for (;;) {
        asm volatile("" ::: "memory");
        const u64 gs = ld_g(sp);
#pragma unroll
        for (int i = 0; i < 4; ++i) t[i] = __builtin_amdgcn_raw_buffer_load_b128(wsr, (unsigned)(WS_QABS + b * 32768 + (4 * wave + i) * 1024 + 16 * lane), 0, 16);
        bool ok = gran_ok(gs, tagsh);
#pragma unroll
        for (int i = 0; i < 4; ++i) {
          const unsigned cs = cs_fold(x32_sumu(x16_sumu((t[i][0] + t[i][1]) + (t[i][2] + t[i][3]))));
          const unsigned want = (unsigned)__shfl((int)(unsigned)gs, 16 * i + c) & 0xFFFFFFu;
          ok = ok && cs == want;
        }
        if (__all(ok)) break;
        if (++spins > SPIN_CAP) break;
        __builtin_amdgcn_s_sleep(1);
      }
#pragma unroll
      for (int i = 0; i < 4; ++i) *(u32x4*)(aux + (4 * wave + i) * 1024 + 16 * lane) = u32x4{t[i][0] ^ key, t[i][1] ^ key, t[i][2] ^ key, t[i][3] ^ key};
    }
    u32x4 stg[16];
    const unsigned soff = (unsigned)((128 + 16 * wave) * 1024 + 16 * lane);
#define LDSTG(I) asm volatile("global_load_dwordx4 %0, %1, %2 offset:%3 nt" : "=v"(stg[I]) : "v"(soff + ((I) >> 2) * 4096u), "s"(crow), "i"(((I) & 3) * 1024) : "memory")
    LDSTG(0); LDSTG(1); LDSTG(2); LDSTG(3); LDSTG(4); LDSTG(5); LDSTG(6); LDSTG(7);
    __syncthreads();
    const int kp = wave >> 1, hh = wave & 1;
    bf16x8 qf[16];
#pragma unroll
    for (int s = 0; s < 16; ++s) qf[s] = *(const bf16x8*)(aux + (hh * 16 + s) * 1024 + 16 * lane);
    __syncthreads();

    char* Pb = aux;
    float* tmax = (float*)(aux + 8192);
    float* tsum = (float*)(aux + 8192 + 512);
    unsigned* csum = (unsigned*)(aux + 8192 + 1024);
    if (tid < 64) csum[tid] = 0u;
    f32x4 acc[4][2];
#pragma unroll
    for (int i = 0; i < 4; ++i) { acc[i][0] = f32x4{0.f, 0.f, 0.f, 0.f}; acc[i][1] = f32x4{0.f, 0.f, 0.f, 0.f}; }
    float Mr0 = -INFINITY, Mr1 = -INFINITY, lsum = 0.f;
    {
      f32x4 s0 = {0.f, 0.f, 0.f, 0.f}, s1 = {0.f, 0.f, 0.f, 0.f};
      {
        const int f = swz(c);
        const char* rb = smem + (32 * kp + c) * 1024;
        bf16x8 fa[2][4];
#define S_LOAD(B, H) do { _Pragma("unroll") for (int j = 0; j < 2; ++j) { const int s_ = 2 * (H) + j; \
          fa[B][2 * j] = *(const bf16x8*)(rb + 256 * (s_ >> 2) + 16 * ((4 * (s_ & 3) + g) ^ f)); fa[B][2 * j + 1] = *(const bf16x8*)(rb + 16384 + 256 * (s_ >> 2) + 16 * ((4 * (s_ & 3) + g) ^ f)); } } while (0)
        S_LOAD(0, 0);
#pragma unroll
        for (int hb = 0; hb < 8; ++hb) {
          if (hb < 7) S_LOAD((hb + 1) & 1, hb + 1);
          __builtin_amdgcn_sched_barrier(0);
#pragma unroll
          for (int j = 0; j < 2; ++j) { s0 = mfma16(fa[hb & 1][2 * j], qf[2 * hb + j], s0); s1 = mfma16(fa[hb & 1][2 * j + 1], qf[2 * hb + j], s1); }
          __builtin_amdgcn_sched_barrier(0);
        }
      }
      LDSTG(8); LDSTG(9); LDSTG(10); LDSTG(11); LDSTG(12); LDSTG(13); LDSTG(14); LDSTG(15);
      float sc[8];
#pragma unroll
      for (int j = 0; j < 4; ++j) { sc[j] = score_round(s0[j]); sc[4 + j] = score_round(s1[j]); }
      float tm = fmaxf(fmaxf(fmaxf(sc[0], sc[1]), fmaxf(sc[2], sc[3])), fmaxf(fmaxf(sc[4], sc[5]), fmaxf(sc[6], sc[7])));
      tm = xrow_max(tm);
      if (g == 0) tmax[kp * 32 + 16 * hh + c] = tm;
      __syncthreads();
      const float Mn0 = fmaxf(Mr0, fmaxf(fmaxf(tmax[c], tmax[32 + c]), fmaxf(tmax[64 + c], tmax[96 + c])));
      const float Mn1 = fmaxf(Mr1, fmaxf(fmaxf(tmax[16 + c], tmax[48 + c]), fmaxf(tmax[80 + c], tmax[112 + c])));
      const float al0 = ex2((Mr0 - Mn0) * LOG2E), al1 = ex2((Mr1 - Mn1) * LOG2E);
      Mr0 = Mn0; Mr1 = Mn1;
      const float Mh = hh ? Mn1 : Mn0;
      {
        float p[8];
#pragma unroll
        for (int j = 0; j < 8; ++j) p[j] = ex2((sc[j] - Mh) * LOG2E);
        lsum = lsum * (hh ? al1 : al0) + (((p[0] + p[1]) + (p[2] + p[3])) + ((p[4] + p[5]) + (p[6] + p[7])));
#pragma unroll
        for (int t = 0; t < 2; ++t)
          *(u32x2*)(Pb + (16 * hh + c) * 256 + 16 * ((2 * (2 * kp + t) + (g >> 1)) ^ swz(c)) + 8 * (g & 1)) = u32x2{pack2(p[4 * t], p[4 * t + 1]), pack2(p[4 * t + 2], p[4 * t + 3])};
      }
#pragma unroll
      for (int ct = 0; ct < 4; ++ct) { acc[ct][0] *= al0; acc[ct][1] *= al1; }
      __syncthreads();
      {
        bf16x8 pa[2][4], pb[2][2];
#define PV_LOAD(B, KS) do { pb[B][0] = *(const bf16x8*)(Pb + c * 256 + 16 * ((4 * (KS) + g) ^ swz(c))); pb[B][1] = *(const bf16x8*)(Pb + (16 + c) * 256 + 16 * ((4 * (KS) + g) ^ swz(c))); \
          _Pragma("unroll") for (int ct = 0; ct < 4; ++ct) pa[B][ct] = tr_frag<1024>(smem, 4 * wave + ct, (KS), lane); } while (0)
#pragma unroll
        for (int ks = 0; ks < 4; ++ks) {
          PV_LOAD(0, ks);
          __builtin_amdgcn_sched_barrier(0);
#pragma unroll
          for (int ct = 0; ct < 4; ++ct) { acc[ct][0] = mfma16(pa[0][ct], pb[0][0], acc[ct][0]); acc[ct][1] = mfma16(pa[0][ct], pb[0][1], acc[ct][1]); }
          __builtin_amdgcn_sched_barrier(0);
        }
      }
    }
    asm volatile("s_waitcnt vmcnt(0)" : "+v"(stg[0]), "+v"(stg[1]), "+v"(stg[2]), "+v"(stg[3]), "+v"(stg[4]), "+v"(stg[5]), "+v"(stg[6]), "+v"(stg[7]),
                 "+v"(stg[8]), "+v"(stg[9]), "+v"(stg[10]), "+v"(stg[11]), "+v"(stg[12]), "+v"(stg[13]), "+v"(stg[14]), "+v"(stg[15]) :: "memory");
    __syncthreads();
#pragma unroll
    for (int i = 0; i < 16; ++i) { const int row = 16 * wave + i; *(u32x4*)(smem + row * 1024 + 16 * (lane ^ swz(row))) = stg[i]; }
    __syncthreads();
    {
      f32x4 s0 = {0.f, 0.f, 0.f, 0.f}, s1 = {0.f, 0.f, 0.f, 0.f};
      {
        const int f = swz(c);
        const char* rb = smem + (32 * kp + c) * 1024;
        bf16x8 fa[2][4];
#define S_LOAD(B, H) do { _Pragma("unroll") for (int j = 0; j < 2; ++j) { const int s_ = 2 * (H) + j; \
          fa[B][2 * j] = *(const bf16x8*)(rb + 256 * (s_ >> 2) + 16 * ((4 * (s_ & 3) + g) ^ f)); fa[B][2 * j + 1] = *(const bf16x8*)(rb + 16384 + 256 * (s_ >> 2) + 16 * ((4 * (s_ & 3) + g) ^ f)); } } while (0)
        S_LOAD(0, 0);
#pragma unroll
        for (int hb = 0; hb < 8; ++hb) {
          if (hb < 7) S_LOAD((hb + 1) & 1, hb + 1);
          __builtin_amdgcn_sched_barrier(0);
#pragma unroll
          for (int j = 0; j < 2; ++j) { s0 = mfma16(fa[hb & 1][2 * j], qf[2 * hb + j], s0); s1 = mfma16(fa[hb & 1][2 * j + 1], qf[2 * hb + j], s1); }
          __builtin_amdgcn_sched_barrier(0);
        }
      }
      float sc[8];
#pragma unroll
      for (int j = 0; j < 4; ++j) { sc[j] = score_round(s0[j]); sc[4 + j] = score_round(s1[j]); }
      float tm = fmaxf(fmaxf(fmaxf(sc[0], sc[1]), fmaxf(sc[2], sc[3])), fmaxf(fmaxf(sc[4], sc[5]), fmaxf(sc[6], sc[7])));
      tm = xrow_max(tm);
      if (g == 0) tmax[kp * 32 + 16 * hh + c] = tm;
      __syncthreads();
      const float Mn0 = fmaxf(Mr0, fmaxf(fmaxf(tmax[c], tmax[32 + c]), fmaxf(tmax[64 + c], tmax[96 + c])));
      const float Mn1 = fmaxf(Mr1, fmaxf(fmaxf(tmax[16 + c], tmax[48 + c]), fmaxf(tmax[80 + c], tmax[112 + c])));
      const float al0 = ex2((Mr0 - Mn0) * LOG2E), al1 = ex2((Mr1 - Mn1) * LOG2E);
      Mr0 = Mn0; Mr1 = Mn1;
      const float Mh = hh ? Mn1 : Mn0;
      {
        float p[8];
#pragma unroll
        for (int j = 0; j < 8; ++j) p[j] = ex2((sc[j] - Mh) * LOG2E);
        lsum = lsum * (hh ? al1 : al0) + (((p[0] + p[1]) + (p[2] + p[3])) + ((p[4] + p[5]) + (p[6] + p[7])));
#pragma unroll
        for (int t = 0; t < 2; ++t)
          *(u32x2*)(Pb + (16 * hh + c) * 256 + 16 * ((2 * (2 * kp + t) + (g >> 1)) ^ swz(c)) + 8 * (g & 1)) = u32x2{pack2(p[4 * t], p[4 * t + 1]), pack2(p[4 * t + 2], p[4 * t + 3])};
      }
#pragma unroll
      for (int ct = 0; ct < 4; ++ct) { acc[ct][0] *= al0; acc[ct][1] *= al1; }
      __syncthreads();
      {
        bf16x8 pa[2][4], pb[2][2];
#define PV_LOAD(B, KS) do { pb[B][0] = *(const bf16x8*)(Pb + c * 256 + 16 * ((4 * (KS) + g) ^ swz(c))); pb[B][1] = *(const bf16x8*)(Pb + (16 + c) * 256 + 16 * ((4 * (KS) + g) ^ swz(c))); \
          _Pragma("unroll") for (int ct = 0; ct < 4; ++ct) pa[B][ct] = tr_frag<1024>(smem, 4 * wave + ct, (KS), lane); } while (0)
        PV_LOAD(0, 0);
#pragma unroll
        for (int ks = 0; ks < 4; ++ks) {
          if (ks < 3) PV_LOAD((ks + 1) & 1, ks + 1);
          __builtin_amdgcn_sched_barrier(0);
#pragma unroll
          for (int ct = 0; ct < 4; ++ct) { acc[ct][0] = mfma16(pa[ks & 1][ct], pb[ks & 1][0], acc[ct][0]); acc[ct][1] = mfma16(pa[ks & 1][ct], pb[ks & 1][1], acc[ct][1]); }
          __builtin_amdgcn_sched_barrier(0);
        }
      }
    }
    { const float ls = xrow_sum(lsum); if (g == 0) tsum[kp * 32 + 16 * hh + c] = ls; }
#pragma unroll
    for (int ht = 0; ht < 2; ++ht) {
      unsigned cs = 0;
#pragma unroll
      for (int cp = 0; cp < 2; ++cp) {
        const u32x4 v = {pack2(acc[2 * cp][ht][0], acc[2 * cp][ht][1]) ^ key, pack2(acc[2 * cp][ht][2], acc[2 * cp][ht][3]) ^ key,
                         pack2(acc[2 * cp + 1][ht][0], acc[2 * cp + 1][ht][1]) ^ key, pack2(acc[2 * cp + 1][ht][2], acc[2 * cp + 1][ht][3]) ^ key};
        __builtin_amdgcn_raw_buffer_store_b128(v, wsr, (unsigned)(WS_APART + (((size_t)b * 32 + 16 * ht + c) * 32 + ck) * 1024 + 128 * wave + 64 * cp + 16 * g), 0, 16);
        cs += (v[0] + v[1]) + (v[2] + v[3]);
      }
      cs = x32_sumu(x16_sumu(cs));
      if (g == 0) __hip_atomic_fetch_add((LAS unsigned*)(csum + (wave >> 2) * 32 + 16 * ht + c), cs, __ATOMIC_RELAXED, __HIP_MEMORY_SCOPE_WORKGROUP);
    }
    RAWBAR();
    if (wave < 2) {
      const int head = 16 * hh + c;
      const float L = (tsum[head] + tsum[32 + head]) + (tsum[64 + head] + tsum[96 + head]);
      const u64 gv = g == 0 ? mk_gran(tagsh, hh ? Mr1 : Mr0) : (g == 1 ? mk_gran(tagsh, L) : (tagsh | (u64)cs_fold(csum[(g - 2) * 32 + head])));
      st_g((u64*)(ws + WS_ASTAT) + (((size_t)head * 2 + b) * 32 + ck) * 4 + g, gv);
    }
  }
}

extern "C" void kernel_launch(void* const* d_in, const int* in_sizes, int n_in, void* d_out, int out_size, void* d_ws, size_t ws_size, hipStream_t stream) {
  (void)hipFuncSetAttribute((const void*)fused_kernel, hipFuncAttributeMaxDynamicSharedMemorySize, LDS_BYTES);
  fused_kernel<<<256, NTHR, LDS_BYTES, stream>>>((const u16*)d_in[0], (const u16*)d_in[1], (const u16*)d_in[2], (const u16*)d_in[3], (const u16*)d_in[4],
      (const u16*)d_in[5], (const u16*)d_in[6], (const u16*)d_in[7], (const u16*)d_in[8], (const u16*)d_in[9], (const u16*)d_in[10], (u16*)d_out, (char*)d_ws);
}
```
